```python
import torch

def _softmax(x, axis=-1):
    return torch.softmax(x, dim=axis)

def _gelu(x):
    return torch.nn.functional.gelu(x, approximate='tanh')

def _silu(x):
    return torch.nn.functional.silu(x)

def _sigmoid(x):
    return torch.sigmoid(x)

def _layernorm(x, axis=-1, eps=1e-05):
    m = x.mean(dim=axis, keepdim=True)
    v = x.var(dim=axis, keepdim=True, unbiased=False)
    return (x - m) / torch.sqrt(v + eps)

def _scatter_add(tgt, idx, src, axis=0):
    return tgt.clone().index_add_(axis, idx, src)

def _segment_sum(src, seg, n):
    out = torch.zeros((n,) + src.shape[1:], dtype=src.dtype)
    return out.index_add_(0, seg, src)

def _pad(x, pw):
    flat = tuple((v for pair in reversed(pw) for v in pair))
    return torch.nn.functional.pad(x, flat)
kernel_name = 'adaLN_cond__sliding_local_dilated__dense_activation_relu__prompt'

def setup_inputs():

    _w = lambda a: (lambda t: t.to(torch.bfloat16) if t.is_floating_point() else t)(torch.as_tensor(a))

    def _A_setup():
        B, S, D = (1, 2048, 512)
        return {'x': _w((lambda *s: torch.randn(*s, dtype=torch.bfloat16) * 0.05)(B, S, D)), 'wqkv': _w((lambda *s: torch.randn(*s, dtype=torch.bfloat16) * 0.05)(D, 3 * D)), 'wo': _w((lambda *s: torch.randn(*s, dtype=torch.bfloat16) * 0.05)(D, D))}
    _A_in = _A_setup()

    def _B_setup():
        B, S, D, Dff = (1, 2048, 512, 1376)
        return {'x': _w((lambda *s: torch.randn(*s, dtype=torch.bfloat16) * 0.05)(B, S, D)), 'w1': _w((lambda *s: torch.randn(*s, dtype=torch.bfloat16) * 0.05)(D, Dff)), 'w2': _w((lambda *s: torch.randn(*s, dtype=torch.bfloat16) * 0.05)(Dff, D))}
    _B_in = _B_setup()
    return {'A_x': _A_in['x'], 'A_wqkv': _A_in['wqkv'], 'A_wo': _A_in['wo'], 'B_x': _B_in['x'], 'B_w1': _B_in['w1'], 'B_w2': _B_in['w2'], 'c': _w((lambda *s: torch.randn(*s, dtype=torch.bfloat16) * 0.1)(1, 512) * 8.0), 'w_mod': _w((lambda *s: torch.randn(*s, dtype=torch.bfloat16) * 0.1)(512, 6 * 512) * 512 ** (-0.5) * 8.0)}

def reference(A_x, A_wqkv, A_wo, B_x, B_w1, B_w2, c, w_mod):

    def _A_ref(x, wqkv, wo):
        B, S, D = x.shape
        H = 32
        Dh = D // H
        W, dil = (8, 2)
        qkv = (lambda x, *d: x.permute(*d))((x @ wqkv).reshape(B, S, 3, H, Dh), 2, 0, 3, 1, 4)
        q, k, v = (qkv[0], qkv[1], qkv[2])
        scale = Dh ** (-0.5)
        pos = torch.arange(S)
        off = torch.arange(W) * dil
        idx = pos[:, None] - off[None, :]
        mask = idx >= 0
        idx_c = torch.where(mask, idx, 0).reshape(-1)
        kg = k[:, :, idx_c, :].reshape(B, H, S, W, Dh)
        vg = v[:, :, idx_c, :].reshape(B, H, S, W, Dh)
        logits = torch.einsum('bhsd,bhswd->bhsw', q, kg) * scale
        logits = torch.where(mask[None, None, :, :], logits, -1000000000.0)
        attn = _softmax(logits, axis=-1)
        o = torch.einsum('bhsw,bhswd->bhsd', attn, vg)
        return (lambda x, *d: x.permute(*d))(o, 0, 2, 1, 3).reshape(B, S, D) @ wo

    def _B_ref(x, w1, w2):
        h = torch.einsum('bsd,df->bsf', x, w1)
        zero = torch.zeros_like(h)
        h = torch.maximum(h, zero)
        return torch.einsum('bsf,fd->bsd', h, w2)
    x = A_x
    mod = (_silu(c) @ w_mod).reshape(c.shape[0], 6, 1, -1)
    sa, ca, ga, sb, cb, gb = (mod[:, 0].clone(), mod[:, 1].clone(), mod[:, 2].clone(), mod[:, 3].clone(), mod[:, 4].clone(), mod[:, 5].clone())
    h = _layernorm(x, axis=-1) * (1 + ca) + sa
    x = x + ga * _A_ref(h, A_wqkv, A_wo)
    h = _layernorm(x, axis=-1) * (1 + cb) + sb
    x = x + gb * _B_ref(h, B_w1, B_w2)
    return x
```

```cpp
#include <hip/hip_runtime.h>
#include <hip/hip_bf16.h>
#include <stdint.h>

#define GAS __attribute__((address_space(1)))
#define LAS __attribute__((address_space(3)))
typedef unsigned v4u __attribute__((ext_vector_type(4)));
typedef unsigned v2u __attribute__((ext_vector_type(2)));
typedef float f32x4 __attribute__((ext_vector_type(4)));
typedef float f32x2 __attribute__((ext_vector_type(2)));
typedef __bf16 bf16x8 __attribute__((ext_vector_type(8)));
typedef short s16x4 __attribute__((ext_vector_type(4)));
typedef __bf16 bf16x2 __attribute__((ext_vector_type(2)));

constexpr int S = 2048, D = 512, DFF = 1376;
constexpr int NC = 32;
constexpr int NF_P1 = 51, NF_P2 = 16, NF_P3 = 51, NF_P4 = 43;
constexpr int F_P1 = 0, F_P2 = F_P1 + NF_P1, F_P3 = F_P2 + NF_P2, F_P4 = F_P3 + NF_P3, NFRAG = F_P4 + NF_P4;
constexpr int KS4 = 43;
constexpr size_t WS_W = 0;
constexpr size_t WS_MOD = WS_W + (size_t)32 * NFRAG * 1024;
constexpr size_t WS_MEAN = WS_MOD + 4096;
constexpr size_t WS_RSTD = WS_MEAN + 2 * 1040 * 4;
constexpr size_t WS_OF = (WS_RSTD + 2 * 1040 * 4 + 4095) / 4096 * 4096;
constexpr size_t WS_X1F = WS_OF + (size_t)NC * 65536;
constexpr size_t WS_ST2 = WS_X1F + (size_t)NC * 65536;
constexpr size_t WS_H1F = WS_ST2 + (size_t)NC * 4096;
constexpr size_t WS_END = WS_H1F + (size_t)NC * KS4 * 4096;

__device__ __forceinline__ float bf2f(unsigned short b) { return __builtin_bit_cast(float, (unsigned)b << 16); }
__device__ __forceinline__ float bflo(unsigned u) { return __builtin_bit_cast(float, u << 16); }
__device__ __forceinline__ float bfhi(unsigned u) { return __builtin_bit_cast(float, u & 0xffff0000u); }
__device__ __forceinline__ unsigned pk2(float lo, float hi) {
  bf16x2 v = {(__bf16)lo, (__bf16)hi};
  return __builtin_bit_cast(unsigned, v);
}
__device__ __forceinline__ float rbf(float f) { return bflo(pk2(f, 0.f)); }
__device__ __forceinline__ f32x4 mfma32(v4u a, v4u b, f32x4 c) {
  return __builtin_amdgcn_mfma_f32_16x16x32_bf16(__builtin_bit_cast(bf16x8, a), __builtin_bit_cast(bf16x8, b), c, 0, 0, 0);
}
__device__ __forceinline__ f32x4 mfma16(v2u a, v2u b, f32x4 c) {
  return __builtin_amdgcn_mfma_f32_16x16x16bf16_1k(__builtin_bit_cast(s16x4, a), __builtin_bit_cast(s16x4, b), c, 0, 0, 0);
}
__device__ __forceinline__ float red16_max(float x) {
  unsigned u = __builtin_bit_cast(unsigned, x);
  auto r = __builtin_amdgcn_permlane16_swap(u, u, false, false);
  const unsigned r0 = r[0], r1 = r[1];
  return fmaxf(__builtin_bit_cast(float, r0), __builtin_bit_cast(float, r1));
}
__device__ __forceinline__ float red32_max(float x) {
  unsigned u = __builtin_bit_cast(unsigned, x);
  auto r = __builtin_amdgcn_permlane32_swap(u, u, false, false);
  const unsigned r0 = r[0], r1 = r[1];
  return fmaxf(__builtin_bit_cast(float, r0), __builtin_bit_cast(float, r1));
}
__device__ __forceinline__ float red16_add(float x) {
  unsigned u = __builtin_bit_cast(unsigned, x);
  auto r = __builtin_amdgcn_permlane16_swap(u, u, false, false);
  const unsigned r0 = r[0], r1 = r[1];
  return __builtin_bit_cast(float, r0) + __builtin_bit_cast(float, r1);
}
__device__ __forceinline__ float red32_add(float x) {
  unsigned u = __builtin_bit_cast(unsigned, x);
  auto r = __builtin_amdgcn_permlane32_swap(u, u, false, false);
  const unsigned r0 = r[0], r1 = r[1];
  return __builtin_bit_cast(float, r0) + __builtin_bit_cast(float, r1);
}
__device__ __forceinline__ v4u pair_swap(v2u a, v2u b) {
  auto r0 = __builtin_amdgcn_permlane16_swap(a.x, b.x, false, false);
  auto r1 = __builtin_amdgcn_permlane16_swap(a.y, b.y, false, false);
  return v4u{r0[0], r1[0], r0[1], r1[1]};
}

struct Ptrs {
  const unsigned short* x; const unsigned short* wqkv; const unsigned short* wo; const unsigned short* w1; const unsigned short* w2;
  const unsigned short* c; const unsigned short* wmod; unsigned short* out; unsigned char* ws;
};

__global__ void __launch_bounds__(256) k_prep(Ptrs p) {
  __shared__ __attribute__((aligned(16))) unsigned short tile[32 * 200];
  __shared__ float sc[512];
  __shared__ float red[4][64];
  __shared__ float modv[64];
  const int b = blockIdx.x, tid = threadIdx.x, lane = tid & 63, wv = tid >> 6;
  unsigned char* ws = p.ws;
  sc[tid] = 0.f;
  {
    float c0 = bf2f(p.c[tid]), c1 = bf2f(p.c[tid + 256]);
    sc[tid] = rbf(c0 / (1.f + __expf(-c0)));
    sc[tid + 256] = rbf(c1 / (1.f + __expf(-c1)));
  }
  if (b >= 256) {
    const int q = b - 256;
    const int col0 = (q < 16 ? 1024 + 32 * q : 2560 + 32 * (q - 16));
    const int ch = tid & 3, r0 = tid >> 2;
    v4u wv4[8];
#pragma unroll
    for (int i = 0; i < 8; ++i) wv4[i] = *(const GAS v4u*)(p.wmod + (size_t)(r0 + 64 * i) * 3072 + col0 + ch * 8);
    __syncthreads();
    float acc[8] = {};
#pragma unroll
    for (int i = 0; i < 8; ++i) {
      const float s = sc[r0 + 64 * i];
#pragma unroll
      for (int e = 0; e < 4; ++e) { acc[2 * e] += s * bflo(wv4[i][e]); acc[2 * e + 1] += s * bfhi(wv4[i][e]); }
    }
#pragma unroll
    for (int e = 0; e < 8; ++e) {
      float v = acc[e];
      v += __shfl_xor(v, 4); v += __shfl_xor(v, 8); v += __shfl_xor(v, 16); v += __shfl_xor(v, 32);
      if (lane < 4) red[wv][lane * 8 + e] = v;
    }
    __syncthreads();
    if (tid < 32) {
      const float v = rbf(red[0][tid] + red[1][tid] + red[2][tid] + red[3][tid]);
      ((float*)(ws + WS_MOD))[(q < 16 ? 0 : 512) + 32 * (q & 15) + tid] = v;
    }
    return;
  }
  const int role = b >> 7, ks = (b >> 3) & 15, j = b & 7;
  const int mch = tid & 7, mr0 = tid >> 3;
  const int colS = (role == 0 ? 0 : 1536) + 32 * ks, colC = colS + 512;
  const int mcol = (mch < 4 ? colS + mch * 8 : colC + (mch - 4) * 8);
  v4u wm[16];
#pragma unroll
  for (int i = 0; i < 16; ++i) wm[i] = *(const GAS v4u*)(p.wmod + (size_t)(mr0 + 32 * i) * 3072 + mcol);
  v4u wt[3];
#pragma unroll
  for (int i = 0; i < 3; ++i) {
    const int id = tid + 256 * i, r = id / 24, ch = id % 24;
    if (role == 0) {
      const int seg = ch >> 3;
      wt[i] = *(const GAS v4u*)(p.wqkv + (size_t)(32 * ks + r) * 1536 + seg * 512 + 64 * j + (ch & 7) * 8);
    } else {
      const int col = 176 * j + ch * 8;
      wt[i] = (ch < 22 && col < DFF) ? *(const GAS v4u*)(p.w1 + (size_t)(32 * ks + r) * DFF + col) : v4u{0, 0, 0, 0};
    }
  }
  v4u wu[2];
  int uid[2];
#pragma unroll
  for (int i = 0; i < 2; ++i) {
    const int u = b + 256 * i;
    uid[i] = u;
    const int r = tid >> 3, ch = tid & 7;
    if (u < 128) wu[i] = *(const GAS v4u*)(p.wo + (size_t)(32 * (u >> 3) + r) * 512 + 64 * (u & 7) + ch * 8);
    else if (u < 472) wu[i] = *(const GAS v4u*)(p.w2 + (size_t)(32 * ((u - 128) >> 3) + r) * 512 + 64 * (u & 7) + ch * 8);
    else wu[i] = v4u{0, 0, 0, 0};
  }
  v4u xr[2];
#pragma unroll
  for (int i = 0; i < 2; ++i) xr[i] = *(const GAS v4u*)(p.x + (size_t)(8 * b + 2 * wv + i) * 512 + lane * 8);
  __syncthreads();
  {
    float acc[8] = {};
#pragma unroll
    for (int i = 0; i < 16; ++i) {
      const float s = sc[mr0 + 32 * i];
#pragma unroll
      for (int e = 0; e < 4; ++e) { acc[2 * e] += s * bflo(wm[i][e]); acc[2 * e + 1] += s * bfhi(wm[i][e]); }
    }
#pragma unroll
    for (int e = 0; e < 8; ++e) {
      float v = acc[e];
      v += __shfl_xor(v, 8); v += __shfl_xor(v, 16); v += __shfl_xor(v, 32);
      if (lane < 8) red[wv][lane * 8 + e] = v;
    }
  }
#pragma unroll
  for (int i = 0; i < 3; ++i) {
    const int id = tid + 256 * i, r = id / 24, ch = id % 24;
    *(v4u*)(tile + r * 200 + ch * 8) = wt[i];
  }
  __syncthreads();
  if (tid < 64) {
    float v = rbf(red[0][tid] + red[1][tid] + red[2][tid] + red[3][tid]);
    if (tid >= 32) v = rbf(1.f + v);
    modv[tid] = v;
  }
#pragma unroll
  for (int i = 0; i < 2; ++i) {
    float s = 0.f;
#pragma unroll
    for (int e = 0; e < 4; ++e) s += bflo(xr[i][e]) + bfhi(xr[i][e]);
#pragma unroll
    for (int m = 1; m < 64; m <<= 1) s += __shfl_xor(s, m);
    const float mean = s * (1.f / 512.f);
    float q = 0.f;
#pragma unroll
    for (int e = 0; e < 4; ++e) { const float a = bflo(xr[i][e]) - mean, c = bfhi(xr[i][e]) - mean; q += a * a + c * c; }
#pragma unroll
    for (int m = 1; m < 64; m <<= 1) q += __shfl_xor(q, m);
    const float rstd = 1.f / sqrtf(q * (1.f / 512.f) + 1e-5f);
    const int s_tok = 8 * b + 2 * wv + i, par = s_tok & 1, u = s_tok >> 1;
    if (lane == 0) {
      ((float*)(ws + WS_MEAN))[par * 1040 + 16 + u] = mean;
      ((float*)(ws + WS_RSTD))[par * 1040 + 16 + u] = rstd;
    }
  }
  if (b == 0 && tid < 32) {
    ((float*)(ws + WS_MEAN))[(tid >> 4) * 1040 + (tid & 15)] = 0.f;
    ((float*)(ws + WS_RSTD))[(tid >> 4) * 1040 + (tid & 15)] = 0.f;
  }
  __syncthreads();
  {
    const int n_l = lane & 15, g = lane >> 4;
#pragma unroll
    for (int i = 0; i < 3; ++i) {
      const int cb = role == 0 ? i * 64 + 16 * wv : 16 * (3 * wv + i);
      float wf[8], tp = 0.f, up = 0.f;
      unsigned o[4];
#pragma unroll
      for (int e = 0; e < 8; ++e) wf[e] = bf2f(tile[(8 * g + e) * 200 + cb + n_l]);
#pragma unroll
      for (int e = 0; e < 4; ++e) {
        const int k0 = 8 * g + 2 * e;
        o[e] = pk2(wf[2 * e] * modv[32 + k0], wf[2 * e + 1] * modv[32 + k0 + 1]);
        tp += wf[2 * e] * modv[k0] + wf[2 * e + 1] * modv[k0 + 1];
        up += bflo(o[e]) + bfhi(o[e]);
      }
      tp = red32_add(red16_add(tp));
      up = red32_add(red16_add(up));
      unsigned char* base = ws + WS_W + ((size_t)(j * 4 + wv) * NFRAG + (role == 0 ? F_P1 : F_P3)) * 1024;
      *(v4u*)(base + (size_t)(ks * 3 + i) * 1024 + lane * 16) = v4u{o[0], o[1], o[2], o[3]};
      if (lane < 16) {
        unsigned short* ext = (unsigned short*)(base + (size_t)(48 + i) * 1024);
        ext[((ks >> 3) * 16 + n_l) * 8 + (ks & 7)] = (unsigned short)(pk2(tp, 0.f) & 0xffff);
        ext[((2 + (ks >> 3)) * 16 + n_l) * 8 + (ks & 7)] = (unsigned short)(pk2(up, 0.f) & 0xffff);
      }
    }
  }
#pragma unroll
  for (int i = 0; i < 2; ++i) {
    __syncthreads();
    *(v4u*)(tile + (tid >> 3) * 200 + (tid & 7) * 8) = wu[i];
    __syncthreads();
    const int u = uid[i];
    if (u < 472) {
      const int n_l = lane & 15, g = lane >> 4;
      unsigned o[4];
#pragma unroll
      for (int e = 0; e < 4; ++e)
        o[e] = (unsigned)tile[(8 * g + 2 * e) * 200 + 16 * wv + n_l] | ((unsigned)tile[(8 * g + 2 * e + 1) * 200 + 16 * wv + n_l] << 16);
      const int uj = u & 7;
      const int fr = u < 128 ? F_P2 + (u >> 3) : F_P4 + ((u - 128) >> 3);
      *(v4u*)(ws + WS_W + ((size_t)(uj * 4 + wv) * NFRAG + fr) * 1024 + lane * 16) = v4u{o[0], o[1], o[2], o[3]};
    }
  }
}

__device__ __forceinline__ void blk_map(int& c, int& j) {
  const int b = blockIdx.x;
  const int x = b & 7, r = b >> 3;
  c = (x >> 1) * 8 + (r & 7);
  j = (x & 1) * 4 + (r >> 3);
}

#define DMA16(gptr, ldsoff) __builtin_amdgcn_global_load_lds((const GAS void*)(gptr), (LAS void*)(lds + (ldsoff)), 16, 0, 0)

__global__ void __launch_bounds__(256, 1) k_p1(Ptrs p) {
  extern __shared__ __attribute__((aligned(16))) unsigned char smem[];
  LAS unsigned char* lds = (LAS unsigned char*)smem;
  int c, j; blk_map(c, j);
  const int tid = threadIdx.x, lane = tid & 63, w = __builtin_amdgcn_readfirstlane(tid >> 6), t = lane & 15, g = lane >> 4;
  const int par = c & 1, cb = c >> 1;
  unsigned char* ws = p.ws;
#pragma unroll
  for (int i = 0; i < 20; ++i) {
    const int r = 4 * i + w;
    int u = 64 * cb - 16 + r; u = u < 0 ? 0 : u;
    DMA16(p.x + (size_t)(2 * u + par) * 512 + ((lane ^ (r & 15)) * 8), r * 1024);
  }
  const GAS v4u* wp = (const GAS v4u*)(ws + WS_W + ((size_t)(j * 4 + w) * NFRAG + F_P1) * 1024) + lane;
  v4u wf[51];
#pragma unroll
  for (int i = 0; i < 51; ++i) wf[i] = wp[i * 64];
  const float* meanp = (const float*)(ws + WS_MEAN) + par * 1040 + 64 * cb;
  const float* rstdp = (const float*)(ws + WS_RSTD) + par * 1040 + 64 * cb;
  float mq[5], rq[5]; f32x4 mv[5], rv[5];
#pragma unroll
  for (int mt = 0; mt < 5; ++mt) {
    mq[mt] = meanp[16 * mt + t]; rq[mt] = rstdp[16 * mt + t];
    mv[mt] = *(const f32x4*)(meanp + 16 * mt + 4 * g); rv[mt] = *(const f32x4*)(rstdp + 16 * mt + 4 * g);
  }
  asm volatile("s_waitcnt vmcnt(0)" ::: "memory");
  __syncthreads();
  f32x4 acc[3][5];
#pragma unroll
  for (int a = 0; a < 3; ++a)
#pragma unroll
    for (int m = 0; m < 5; ++m) acc[a][m] = f32x4{0, 0, 0, 0};
#pragma unroll
  for (int ks = 0; ks < 16; ++ks) {
    v4u xf[5];
#pragma unroll
    for (int mt = 0; mt < 5; ++mt)
      xf[mt] = *(const LAS v4u*)(lds + (16 * mt + t) * 1024 + (((4 * ks + g) ^ t) * 16));
#pragma unroll
    for (int mt = 0; mt < 5; ++mt) {
      if (mt > 0) acc[0][mt] = mfma32(wf[ks * 3 + 0], xf[mt], acc[0][mt]);
      acc[1][mt] = mfma32(wf[ks * 3 + 1], xf[mt], acc[1][mt]);
      acc[2][mt] = mfma32(xf[mt], wf[ks * 3 + 2], acc[2][mt]);
    }
  }
  const unsigned one2 = 0x3f803f80u;
  const v4u onesT = g < 2 ? v4u{one2, one2, one2, one2} : v4u{0, 0, 0, 0};
  const v4u onesU = g >= 2 ? v4u{one2, one2, one2, one2} : v4u{0, 0, 0, 0};
  const f32x4 z4 = {0, 0, 0, 0};
  const f32x4 tq = mfma32(wf[48], onesT, z4), uq = mfma32(wf[48], onesU, z4);
  const f32x4 tk = mfma32(wf[49], onesT, z4), uk = mfma32(wf[49], onesU, z4);
  const f32x4 tv = mfma32(onesT, wf[50], z4), uv = mfma32(onesU, wf[50], z4);
  v2u qf[5], kf[5], vf[5];
#pragma unroll
  for (int mt = 0; mt < 5; ++mt) {
    float a[4], bq[4], cv[4];
#pragma unroll
    for (int i = 0; i < 4; ++i) {
      a[i] = rq[mt] * (acc[0][mt][i] - mq[mt] * uq[i]) + tq[i];
      bq[i] = rq[mt] * (acc[1][mt][i] - mq[mt] * uk[i]) + tk[i];
      cv[i] = rv[mt][i] * (acc[2][mt][i] - mv[mt][i] * uv[0]) + tv[0];
    }
    qf[mt] = v2u{pk2(a[0], a[1]), pk2(a[2], a[3])};
    kf[mt] = v2u{pk2(bq[0], bq[1]), pk2(bq[2], bq[3])};
    vf[mt] = v2u{pk2(cv[0], cv[1]), pk2(cv[2], cv[3])};
  }
  const float NEG = -3.0e38f, SC = 0.25f * 1.4426950408889634f;
  v2u of[4];
#pragma unroll
  for (int mt = 1; mt < 5; ++mt) {
    const f32x4 sp = mfma16(kf[mt - 1], qf[mt], z4);
    const f32x4 sc = mfma16(kf[mt], qf[mt], z4);
    const bool prev_ok = !(mt == 1 && cb == 0);
    float e[8];
#pragma unroll
    for (int i = 0; i < 4; ++i) {
      const int kk = 4 * g + i;
      e[i] = (prev_ok && kk >= t + 9) ? sp[i] : NEG;
      e[4 + i] = (kk <= t && t - kk <= 7) ? sc[i] : NEG;
    }
    float m = fmaxf(fmaxf(fmaxf(e[0], e[1]), fmaxf(e[2], e[3])), fmaxf(fmaxf(e[4], e[5]), fmaxf(e[6], e[7])));
    m = red32_max(red16_max(m));
    float sum = 0.f;
#pragma unroll
    for (int i = 0; i < 8; ++i) { e[i] = __builtin_amdgcn_exp2f((e[i] - m) * SC); sum += e[i]; }
    sum = red32_add(red16_add(sum));
    const float inv = 1.f / sum;
    const v2u pp = {pk2(e[0] * inv, e[1] * inv), pk2(e[2] * inv, e[3] * inv)};
    const v2u pc = {pk2(e[4] * inv, e[5] * inv), pk2(e[6] * inv, e[7] * inv)};
    f32x4 o = mfma16(vf[mt - 1], pp, z4);
    o = mfma16(vf[mt], pc, o);
    of[mt - 1] = v2u{pk2(o[0], o[1]), pk2(o[2], o[3])};
  }
  const int h = 4 * j + w;
  unsigned char* ob = ws + WS_OF + (size_t)c * 65536 + (size_t)(h >> 1) * 4096 + (16 * (2 * (h & 1) + (g >> 1)) + t) * 16;
#pragma unroll
  for (int pr = 0; pr < 2; ++pr) {
    const v4u v = pair_swap(of[2 * pr], of[2 * pr + 1]);
    *(v4u*)(ob + (2 * pr + (g & 1)) * 1024) = v;
  }
}

__global__ void __launch_bounds__(256, 1) k_p2(Ptrs p) {
  extern __shared__ __attribute__((aligned(16))) unsigned char smem[];
  LAS unsigned char* lds = (LAS unsigned char*)smem;
  int c, j; blk_map(c, j);
  const int tid = threadIdx.x, lane = tid & 63, w = __builtin_amdgcn_readfirstlane(tid >> 6), t = lane & 15, g = lane >> 4;
  const int par = c & 1, cb = c >> 1;
  unsigned char* ws = p.ws;
  const unsigned char* src = ws + WS_OF + (size_t)c * 65536;
#pragma unroll
  for (int i = 0; i < 16; ++i) DMA16(src + (i * 4 + w) * 1024 + lane * 16, (i * 4 + w) * 1024);
  const GAS v4u* wp = (const GAS v4u*)(ws + WS_W + ((size_t)(j * 4 + w) * NFRAG + F_P2) * 1024) + lane;
  v4u wf[16];
#pragma unroll
  for (int i = 0; i < 16; ++i) wf[i] = wp[i * 64];
  const int n0 = 64 * j + 16 * w + 4 * g;
  v2u xr[4];
#pragma unroll
  for (int mt = 0; mt < 4; ++mt) xr[mt] = *(const v2u*)(p.x + (size_t)(2 * (64 * cb + 16 * mt + t) + par) * 512 + n0);
  const f32x4 ga = *(const f32x4*)((const float*)(ws + WS_MOD) + n0);
  asm volatile("s_waitcnt vmcnt(0)" ::: "memory");
  __syncthreads();
  f32x4 acc[4] = {};
#pragma unroll
  for (int ks = 0; ks < 16; ++ks)
#pragma unroll
    for (int mt = 0; mt < 4; ++mt) {
      const v4u af = *(const LAS v4u*)(lds + (ks * 4 + mt) * 1024 + lane * 16);
      acc[mt] = mfma32(wf[ks], af, acc[mt]);
    }
  v2u xo[4];
  float s1[4], s2[4];
#pragma unroll
  for (int mt = 0; mt < 4; ++mt) {
    const float a0 = bflo(xr[mt].x) + ga[0] * acc[mt][0], a1 = bfhi(xr[mt].x) + ga[1] * acc[mt][1];
    const float a2 = bflo(xr[mt].y) + ga[2] * acc[mt][2], a3 = bfhi(xr[mt].y) + ga[3] * acc[mt][3];
    xo[mt] = v2u{pk2(a0, a1), pk2(a2, a3)};
    const float b0 = bflo(xo[mt].x), b1 = bfhi(xo[mt].x), b2 = bflo(xo[mt].y), b3 = bfhi(xo[mt].y);
    s1[mt] = red32_add(red16_add(b0 + b1 + b2 + b3));
    s2[mt] = red32_add(red16_add(b0 * b0 + b1 * b1 + b2 * b2 + b3 * b3));
  }
  unsigned char* xb = ws + WS_X1F + (size_t)c * 65536 + (size_t)(2 * j + (w >> 1)) * 4096 + (16 * (2 * (w & 1) + (g >> 1)) + t) * 16;
#pragma unroll
  for (int pr = 0; pr < 2; ++pr) {
    const v4u v = pair_swap(xo[2 * pr], xo[2 * pr + 1]);
    *(v4u*)(xb + (2 * pr + (g & 1)) * 1024) = v;
  }
  __syncthreads();
  LAS f32x2* st = (LAS f32x2*)lds;
  if (g == 0) {
#pragma unroll
    for (int mt = 0; mt < 4; ++mt) st[w * 64 + 16 * mt + t] = f32x2{s1[mt], s2[mt]};
  }
  __syncthreads();
  if (tid < 64) {
    const f32x2 v = st[tid] + st[64 + tid] + st[128 + tid] + st[192 + tid];
    *(f32x2*)(ws + WS_ST2 + (size_t)c * 4096 + (j * 64 + tid) * 8) = v;
  }
}

__global__ void __launch_bounds__(256, 1) k_p3(Ptrs p) {
  extern __shared__ __attribute__((aligned(16))) unsigned char smem[];
  LAS unsigned char* lds = (LAS unsigned char*)smem;
  int c, j; blk_map(c, j);
  const int tid = threadIdx.x, lane = tid & 63, w = __builtin_amdgcn_readfirstlane(tid >> 6), t = lane & 15, g = lane >> 4;
  unsigned char* ws = p.ws;
  const unsigned char* src = ws + WS_X1F + (size_t)c * 65536;
#pragma unroll
  for (int i = 0; i < 16; ++i) DMA16(src + (i * 4 + w) * 1024 + lane * 16, (i * 4 + w) * 1024);
  const GAS v4u* wp = (const GAS v4u*)(ws + WS_W + ((size_t)(j * 4 + w) * NFRAG + F_P3) * 1024) + lane;
  v4u wf[51];
#pragma unroll
  for (int i = 0; i < 51; ++i) wf[i] = wp[i * 64];
  {
    const int tok = tid >> 2, part = tid & 3;
    const f32x2 a = *(const f32x2*)(ws + WS_ST2 + (size_t)c * 4096 + ((2 * part) * 64 + tok) * 8);
    const f32x2 b = *(const f32x2*)(ws + WS_ST2 + (size_t)c * 4096 + ((2 * part + 1) * 64 + tok) * 8);
    float s1 = a.x + b.x, s2 = a.y + b.y;
    s1 += __shfl_xor(s1, 1); s1 += __shfl_xor(s1, 2);
    s2 += __shfl_xor(s2, 1); s2 += __shfl_xor(s2, 2);
    const float mean = s1 * (1.f / 512.f);
    const float var = fmaxf(s2 * (1.f / 512.f) - mean * mean, 0.f);
    const float rstd = 1.f / sqrtf(var + 1e-5f);
    if (part == 0) *(LAS f32x2*)(lds + 65536 + tok * 8) = f32x2{mean, rstd};
  }
  asm volatile("s_waitcnt vmcnt(0)" ::: "memory");
  __syncthreads();
  f32x4 acc[3][4] = {};
#pragma unroll
  for (int ks = 0; ks < 16; ++ks) {
    v4u af[4];
#pragma unroll
    for (int mt = 0; mt < 4; ++mt) af[mt] = *(const LAS v4u*)(lds + (ks * 4 + mt) * 1024 + lane * 16);
#pragma unroll
    for (int i = 0; i < 3; ++i)
#pragma unroll
      for (int mt = 0; mt < 4; ++mt) acc[i][mt] = mfma32(wf[ks * 3 + i], af[mt], acc[i][mt]);
  }
  const unsigned one2 = 0x3f803f80u;
  const v4u onesT = g < 2 ? v4u{one2, one2, one2, one2} : v4u{0, 0, 0, 0};
  const v4u onesU = g >= 2 ? v4u{one2, one2, one2, one2} : v4u{0, 0, 0, 0};
  const f32x4 z4 = {0, 0, 0, 0};
  f32x2 st[4];
#pragma unroll
  for (int mt = 0; mt < 4; ++mt) st[mt] = *(const LAS f32x2*)(lds + 65536 + (16 * mt + t) * 8);
#pragma unroll
  for (int i = 0; i < 3; ++i) {
    const f32x4 tt = mfma32(wf[48 + i], onesT, z4), uu = mfma32(wf[48 + i], onesU, z4);
    v2u hv[4];
#pragma unroll
    for (int mt = 0; mt < 4; ++mt) {
      float a[4];
#pragma unroll
      for (int e = 0; e < 4; ++e) a[e] = fmaxf(st[mt].y * (acc[i][mt][e] - st[mt].x * uu[e]) + tt[e], 0.f);
      hv[mt] = v2u{pk2(a[0], a[1]), pk2(a[2], a[3])};
    }
    const int tl = 3 * w + i, T = 11 * j + tl;
    const v4u v0 = pair_swap(hv[0], hv[1]), v1 = pair_swap(hv[2], hv[3]);
    if (tl < 11 && T < 86) {
      unsigned char* hb = ws + WS_H1F + (size_t)c * (KS4 * 4096) + (size_t)(T >> 1) * 4096 + (16 * (2 * (T & 1) + (g >> 1)) + t) * 16;
      *(v4u*)(hb + (0 + (g & 1)) * 1024) = v0;
      *(v4u*)(hb + (2 + (g & 1)) * 1024) = v1;
    }
  }
}

__global__ void __launch_bounds__(256, 1) k_p4(Ptrs p) {
  extern __shared__ __attribute__((aligned(16))) unsigned char smem[];
  LAS unsigned char* lds = (LAS unsigned char*)smem;
  int c, j; blk_map(c, j);
  const int tid = threadIdx.x, lane = tid & 63, w = __builtin_amdgcn_readfirstlane(tid >> 6), t = lane & 15, g = lane >> 4;
  const int par = c & 1, cb = c >> 1;
  unsigned char* ws = p.ws;
  const unsigned char* src = ws + WS_H1F + (size_t)c * (KS4 * 4096);
#pragma unroll
  for (int i = 0; i < 39; ++i) DMA16(src + (i * 4 + w) * 1024 + lane * 16, (i * 4 + w) * 1024);
  const GAS v4u* wp = (const GAS v4u*)(ws + WS_W + ((size_t)(j * 4 + w) * NFRAG + F_P4) * 1024) + lane;
  v4u wf[43];
#pragma unroll
  for (int i = 0; i < 43; ++i) wf[i] = wp[i * 64];
  const int n0 = 64 * j + 16 * w + 4 * g;
  v2u xr[4];
#pragma unroll
  for (int mt = 0; mt < 4; ++mt)
    xr[mt] = *(const v2u*)(ws + WS_X1F + (size_t)c * 65536 + (size_t)(2 * j + (w >> 1)) * 4096 + mt * 1024 +
                           (16 * (2 * (w & 1) + (g >> 1)) + t) * 16 + (g & 1) * 8);
  const f32x4 gb = *(const f32x4*)((const float*)(ws + WS_MOD) + 512 + n0);
  asm volatile("s_waitcnt vmcnt(0)" ::: "memory");
  __syncthreads();
  f32x4 acc[4] = {};
#pragma unroll
  for (int ks = 0; ks < 39; ++ks) {
#pragma unroll
    for (int mt = 0; mt < 4; ++mt) {
      const v4u af = *(const LAS v4u*)(lds + (ks * 4 + mt) * 1024 + lane * 16);
      acc[mt] = mfma32(wf[ks], af, acc[mt]);
    }
    if (ks == 3) {
      __syncthreads();
#pragma unroll
      for (int i = 0; i < 4; ++i) DMA16(src + ((39 + i) * 4 + w) * 1024 + lane * 16, (i * 4 + w) * 1024);
    }
  }
  asm volatile("s_waitcnt vmcnt(0)" ::: "memory");
  __syncthreads();
#pragma unroll
  for (int ks = 39; ks < 43; ++ks)
#pragma unroll
    for (int mt = 0; mt < 4; ++mt) {
      const v4u af = *(const LAS v4u*)(lds + ((ks - 39) * 4 + mt) * 1024 + lane * 16);
      acc[mt] = mfma32(wf[ks], af, acc[mt]);
    }
  v2u o[4];
#pragma unroll
  for (int mt = 0; mt < 4; ++mt) {
    const float a0 = bflo(xr[mt].x) + gb[0] * acc[mt][0], a1 = bfhi(xr[mt].x) + gb[1] * acc[mt][1];
    const float a2 = bflo(xr[mt].y) + gb[2] * acc[mt][2], a3 = bfhi(xr[mt].y) + gb[3] * acc[mt][3];
    o[mt] = v2u{pk2(a0, a1), pk2(a2, a3)};
  }
#pragma unroll
  for (int pr = 0; pr < 2; ++pr) {
    const v4u v = pair_swap(o[2 * pr], o[2 * pr + 1]);
    const int mt = 2 * pr + (g & 1);
    *(v4u*)(p.out + (size_t)(2 * (64 * cb + 16 * mt + t) + par) * 512 + 64 * j + 16 * w + 8 * (g >> 1)) = v;
  }
}

extern "C" void kernel_launch(void* const* d_in, const int* in_sizes, int n_in, void* d_out, int out_size, void* d_ws,
                              size_t ws_size, hipStream_t stream) {
  Ptrs p;
  p.x = (const unsigned short*)d_in[0]; p.wqkv = (const unsigned short*)d_in[1]; p.wo = (const unsigned short*)d_in[2];
  p.w1 = (const unsigned short*)d_in[4]; p.w2 = (const unsigned short*)d_in[5];
  p.c = (const unsigned short*)d_in[6]; p.wmod = (const unsigned short*)d_in[7];
  p.out = (unsigned short*)d_out; p.ws = (unsigned char*)d_ws;
  hipFuncSetAttribute((const void*)k_p1, hipFuncAttributeMaxDynamicSharedMemorySize, 81920);
  hipFuncSetAttribute((const void*)k_p2, hipFuncAttributeMaxDynamicSharedMemorySize, 65536);
  hipFuncSetAttribute((const void*)k_p3, hipFuncAttributeMaxDynamicSharedMemorySize, 65536 + 512);
  hipFuncSetAttribute((const void*)k_p4, hipFuncAttributeMaxDynamicSharedMemorySize, 39 * 4096);
  k_prep<<<288, 256, 0, stream>>>(p);
  k_p1<<<256, 256, 81920, stream>>>(p);
  k_p2<<<256, 256, 65536, stream>>>(p);
  k_p3<<<256, 256, 65536 + 512, stream>>>(p);
  k_p4<<<256, 256, 39 * 4096, stream>>>(p);
}
```

```cpp
#include <hip/hip_runtime.h>
#include <hip/hip_bf16.h>
#include <stdint.h>

constexpr size_t WS_W = 0, WS_MOD = 5275648, WS_MEAN = 5279744, WS_RSTD = 5288064, WS_FLAG = 5300224, WS_OF = 5332992,
                 WS_X1F = 7954432, WS_ST2 = 10575872, WS_H1F = 11100160, WS_END = 18309120, WS_DBG = 134217728;
constexpr int LDS_ST1 = 159744, LDS_BYTES = 163840;
#define STAMPS 0

struct Ptrs {
  const void* x; const void* wqkv; const void* wo; const void* w1; const void* w2; const void* c; const void* wmod; void* out; void* ws;
};

__global__ void __launch_bounds__(256, 1) k_mega(Ptrs p) {
  asm volatile(
      "; set-up\n"
      "s_load_dwordx16 s[64:79], %0, 0x0\n"
      "s_load_dwordx2 s[80:81], %0, 0x40\n"
      "s_mov_b32 s34, %1\n"
      "v_mov_b32 v4, %2\n"
      "v_and_b32 v7, 63, v4\n"
      "v_lshlrev_b32 v5, 4, v7\n"
      "v_lshrrev_b32 v48, 6, v4\n"
      "s_nop 0\n"
      "v_readfirstlane_b32 s30, v48\n"
      "s_and_b32 s83, s34, 7\n"
      "s_lshr_b32 s84, s34, 3\n"
      "s_lshr_b32 s28, s83, 1\n"
      "s_lshl_b32 s28, s28, 3\n"
      "s_and_b32 s85, s84, 7\n"
      "s_add_u32 s28, s28, s85\n"
      "s_and_b32 s29, s83, 1\n"
      "s_lshl_b32 s29, s29, 2\n"
      "s_lshr_b32 s85, s84, 3\n"
      "s_add_u32 s29, s29, s85\n"
      "s_lshl_b32 s36, s83, 1\n"
      "s_and_b32 s85, s84, 1\n"
      "s_add_u32 s36, s36, s85\n"
      "s_lshr_b32 s35, s84, 1\n"
      "s_and_b32 s35, s35, 1\n"
      "s_lshr_b32 s39, s84, 2\n"
      "s_lshl_b32 s96, s83, 2\n"
      "s_and_b32 s85, s84, 3\n"
      "s_add_u32 s96, s96, s85\n"
      "s_and_b32 s38, s28, 1\n"
      "s_lshr_b32 s37, s28, 1\n"
      "v_and_b32 v48, 15, v7\n"
      "v_lshrrev_b32 v49, 4, v7\n"
      "v_bfe_u32 v50, v7, 1, 2\n"
      "v_and_b32 v51, 1, v49\n"
      "v_xor_b32 v50, v50, v51\n"
      "v_lshrrev_b32 v52, 3, v7\n"
      "v_mul_u32_u24 v52, 6144, v52\n"
      "v_and_b32 v51, 1, v50\n"
      "v_lshl_add_u32 v52, v51, 5, v52\n"
      "v_lshrrev_b32 v51, 1, v50\n"
      "v_lshl_add_u32 v52, v51, 10, v52\n"
      "v_and_b32 v51, 1, v7\n"
      "v_lshl_add_u32 v52, v51, 4, v52\n"
      "v_xor_b32 v54, 2, v50\n"
      "v_lshrrev_b32 v53, 3, v7\n"
      "v_mul_u32_u24 v53, 6144, v53\n"
      "v_and_b32 v51, 1, v54\n"
      "v_lshl_add_u32 v53, v51, 5, v53\n"
      "v_lshrrev_b32 v51, 1, v54\n"
      "v_lshl_add_u32 v53, v51, 10, v53\n"
      "v_and_b32 v51, 1, v7\n"
      "v_lshl_add_u32 v53, v51, 4, v53\n"
      "s_waitcnt lgkmcnt(0)\n"
      "s_mov_b32 s20, s80\n"
      "s_and_b32 s21, s81, 0xffff\n"
      "s_mov_b32 s22, 0x10000000\n"
      "s_mov_b32 s23, 0x20000\n"
      "s_mov_b32 s88, s76\n"
      "s_and_b32 s89, s77, 0xffff\n"
      "s_mov_b32 s90, 0x300000\n"
      "s_mov_b32 s91, 0x20000\n"
      "s_mov_b32 s92, s74\n"
      "s_and_b32 s93, s75, 0xffff\n"
      "s_mov_b32 s94, 0x400\n"
      "s_mov_b32 s95, 0x20000\n"
      "v_lshlrev_b32 v51, 2, v4\n"
      "buffer_load_dword v55, v51, s[92:95], 0 offen\n"
      "s_mul_i32 s83, s30, 786432\n"
      "s_mul_i32 s84, s35, 3072\n"
      "s_add_u32 s83, s83, s84\n"
      "s_lshl_b32 s84, s36, 6\n"
      "s_add_u32 s87, s83, s84\n"
      "s_lshl_b32 s84, s30, 14\n"
      "s_add_u32 m0, s84, 81920\n"
      "s_nop 0\n"
      "buffer_load_dwordx4 v52, s[88:91], s87 offen lds\n"
      "s_add_u32 m0, m0, 1024\n"
      "s_add_u32 s87, s87, 49152\n"
      "buffer_load_dwordx4 v53, s[88:91], s87 offen lds\n"
      "s_add_u32 m0, m0, 1024\n"
      "s_add_u32 s87, s87, 49152\n"
      "buffer_load_dwordx4 v52, s[88:91], s87 offen lds\n"
      "s_add_u32 m0, m0, 1024\n"
      "s_add_u32 s87, s87, 49152\n"
      "buffer_load_dwordx4 v53, s[88:91], s87 offen lds\n"
      "s_add_u32 m0, m0, 1024\n"
      "s_add_u32 s87, s87, 49152\n"
      "buffer_load_dwordx4 v52, s[88:91], s87 offen lds\n"
      "s_add_u32 m0, m0, 1024\n"
      "s_add_u32 s87, s87, 49152\n"
      "buffer_load_dwordx4 v53, s[88:91], s87 offen lds\n"
      "s_add_u32 m0, m0, 1024\n"
      "s_add_u32 s87, s87, 49152\n"
      "buffer_load_dwordx4 v52, s[88:91], s87 offen lds\n"
      "s_add_u32 m0, m0, 1024\n"
      "s_add_u32 s87, s87, 49152\n"
      "buffer_load_dwordx4 v53, s[88:91], s87 offen lds\n"
      "s_add_u32 m0, m0, 1024\n"
      "s_add_u32 s87, s87, 49152\n"
      "buffer_load_dwordx4 v52, s[88:91], s87 offen lds\n"
      "s_add_u32 m0, m0, 1024\n"
      "s_add_u32 s87, s87, 49152\n"
      "buffer_load_dwordx4 v53, s[88:91], s87 offen lds\n"
      "s_add_u32 m0, m0, 1024\n"
      "s_add_u32 s87, s87, 49152\n"
      "buffer_load_dwordx4 v52, s[88:91], s87 offen lds\n"
      "s_add_u32 m0, m0, 1024\n"
      "s_add_u32 s87, s87, 49152\n"
      "buffer_load_dwordx4 v53, s[88:91], s87 offen lds\n"
      "s_add_u32 m0, m0, 1024\n"
      "s_add_u32 s87, s87, 49152\n"
      "buffer_load_dwordx4 v52, s[88:91], s87 offen lds\n"
      "s_add_u32 m0, m0, 1024\n"
      "s_add_u32 s87, s87, 49152\n"
      "buffer_load_dwordx4 v53, s[88:91], s87 offen lds\n"
      "s_add_u32 m0, m0, 1024\n"
      "s_add_u32 s87, s87, 49152\n"
      "buffer_load_dwordx4 v52, s[88:91], s87 offen lds\n"
      "s_add_u32 m0, m0, 1024\n"
      "s_add_u32 s87, s87, 49152\n"
      "buffer_load_dwordx4 v53, s[88:91], s87 offen lds\n"
      "s_cmp_eq_u32 s35, 0\n"
      "s_mov_b32 s97, 2752\n"
      "s_cselect_b32 s97, 3072, s97\n"
      "s_cselect_b32 s92, s66, s70\n"
      "s_cselect_b32 s93, s67, s71\n"
      "s_mov_b32 s94, 1409024\n"
      "s_cselect_b32 s94, 1572864, s94\n"
      "s_mov_b32 s98, 352\n"
      "s_cselect_b32 s98, 128, s98\n"
      "s_and_b32 s93, s93, 0xffff\n"
      "s_mul_i32 s83, s36, s97\n"
      "s_lshl_b32 s83, s83, 5\n"
      "s_mul_i32 s84, s39, s98\n"
      "s_add_u32 s87, s83, s84\n"
      "s_mul_i32 s99, s39, 352\n"
      "s_sub_u32 s99, 2752, s99\n"
      "s_min_u32 s99, s99, 352\n"
      "s_mul_i32 s83, s30, 192\n"
      "s_add_u32 s83, s83, 0\n"
      "v_add_u32 v50, s83, v7\n"
      "v_mul_u32_u24 v51, 2731, v50\n"
      "v_lshrrev_b32 v51, 16, v51\n"
      "v_mul_u32_u24 v54, 24, v51\n"
      "v_sub_u32 v54, v50, v54\n"
      "v_mul_lo_u32 v57, v51, s97\n"
      "v_lshrrev_b32 v60, 3, v54\n"
      "v_lshlrev_b32 v60, 10, v60\n"
      "v_and_b32 v61, 7, v54\n"
      "v_lshl_add_u32 v60, v61, 4, v60\n"
      "v_lshlrev_b32 v61, 4, v54\n"
      "v_cmp_gt_u32 vcc, s99, v61\n"
      "v_mov_b32 v54, 0x40000000\n"
      "s_nop 0\n"
      "v_cndmask_b32 v61, v54, v61, vcc\n"
      "s_cmp_eq_u32 s35, 0\n"
      "s_cselect_b64 vcc, -1, 0\n"
      "s_nop 0\n"
      "v_cndmask_b32 v60, v61, v60, vcc\n"
      "v_add_u32 v57, v57, v60\n"
      "s_mul_i32 s83, s30, 192\n"
      "s_add_u32 s83, s83, 64\n"
      "v_add_u32 v50, s83, v7\n"
      "v_mul_u32_u24 v51, 2731, v50\n"
      "v_lshrrev_b32 v51, 16, v51\n"
      "v_mul_u32_u24 v54, 24, v51\n"
      "v_sub_u32 v54, v50, v54\n"
      "v_mul_lo_u32 v58, v51, s97\n"
      "v_lshrrev_b32 v60, 3, v54\n"
      "v_lshlrev_b32 v60, 10, v60\n"
      "v_and_b32 v61, 7, v54\n"
      "v_lshl_add_u32 v60, v61, 4, v60\n"
      "v_lshlrev_b32 v61, 4, v54\n"
      "v_cmp_gt_u32 vcc, s99, v61\n"
      "v_mov_b32 v54, 0x40000000\n"
      "s_nop 0\n"
      "v_cndmask_b32 v61, v54, v61, vcc\n"
      "s_cmp_eq_u32 s35, 0\n"
      "s_cselect_b64 vcc, -1, 0\n"
      "s_nop 0\n"
      "v_cndmask_b32 v60, v61, v60, vcc\n"
      "v_add_u32 v58, v58, v60\n"
      "s_mul_i32 s83, s30, 192\n"
      "s_add_u32 s83, s83, 128\n"
      "v_add_u32 v50, s83, v7\n"
      "v_mul_u32_u24 v51, 2731, v50\n"
      "v_lshrrev_b32 v51, 16, v51\n"
      "v_mul_u32_u24 v54, 24, v51\n"
      "v_sub_u32 v54, v50, v54\n"
      "v_mul_lo_u32 v59, v51, s97\n"
      "v_lshrrev_b32 v60, 3, v54\n"
      "v_lshlrev_b32 v60, 10, v60\n"
      "v_and_b32 v61, 7, v54\n"
      "v_lshl_add_u32 v60, v61, 4, v60\n"
      "v_lshlrev_b32 v61, 4, v54\n"
      "v_cmp_gt_u32 vcc, s99, v61\n"
      "v_mov_b32 v54, 0x40000000\n"
      "s_nop 0\n"
      "v_cndmask_b32 v61, v54, v61, vcc\n"
      "s_cmp_eq_u32 s35, 0\n"
      "s_cselect_b64 vcc, -1, 0\n"
      "s_nop 0\n"
      "v_cndmask_b32 v60, v61, v60, vcc\n"
      "v_add_u32 v59, v59, v60\n"
      "s_mul_i32 s84, s30, 3072\n"
      "s_add_u32 m0, s84, 147456\n"
      "s_nop 0\n"
      "buffer_load_dwordx4 v57, s[92:95], s87 offen lds\n"
      "s_add_u32 m0, m0, 1024\n"
      "s_nop 0\n"
      "buffer_load_dwordx4 v58, s[92:95], s87 offen lds\n"
      "s_add_u32 m0, m0, 1024\n"
      "s_nop 0\n"
      "buffer_load_dwordx4 v59, s[92:95], s87 offen lds\n"
      "v_mov_b32 v52, 0\n"
      "s_mov_b32 s83, 5314560\n"
      "buffer_load_dword v56, v52, s[20:23], s83 offen sc1\n"
      "v_lshrrev_b32 v50, 3, v4\n"
      "v_and_b32 v51, 7, v4\n"
      "v_lshlrev_b32 v51, 4, v51\n"
      "v_lshl_add_u32 v53, v50, 10, v51\n"
      "s_add_u32 s83, s96, 0\n"
      "s_cmp_lt_u32 s83, 16\n"
      "s_cselect_b32 s92, s68, s72\n"
      "s_cselect_b32 s93, s69, s73\n"
      "s_mov_b32 s94, 1409024\n"
      "s_cselect_b32 s94, 524288, s94\n"
      "s_cselect_b32 s84, 0, 16\n"
      "s_and_b32 s93, s93, 0xffff\n"
      "s_sub_u32 s84, s83, s84\n"
      "s_lshl_b32 s84, s84, 15\n"
      "s_lshl_b32 s85, s39, 7\n"
      "s_add_u32 s4, s84, s85\n"
      "buffer_load_dwordx4 v[60:63], v53, s[92:95], s4 offen\n"
      "s_nop 3\n"
      "s_add_u32 s83, s96, 32\n"
      "s_cmp_lt_u32 s83, 16\n"
      "s_cselect_b32 s92, s68, s72\n"
      "s_cselect_b32 s93, s69, s73\n"
      "s_mov_b32 s94, 1409024\n"
      "s_cselect_b32 s94, 524288, s94\n"
      "s_cselect_b32 s84, 0, 16\n"
      "s_and_b32 s93, s93, 0xffff\n"
      "s_sub_u32 s84, s83, s84\n"
      "s_lshl_b32 s84, s84, 15\n"
      "s_lshl_b32 s85, s39, 7\n"
      "s_add_u32 s5, s84, s85\n"
      "buffer_load_dwordx4 v[64:67], v53, s[92:95], s5 offen\n"
      "s_nop 3\n"
      "s_and_b32 s83, s96, 7\n"
      "s_cmp_lg_u32 s83, 0\n"
      "s_cbranch_scc1 Lg0_%=\n"
      "s_lshr_b32 s83, s96, 3\n"
      "s_cmp_lt_u32 s83, 2\n"
      "s_mov_b32 s84, 5120\n"
      "s_cselect_b32 s84, 2048, s84\n"
      "s_and_b32 s83, s83, 1\n"
      "s_lshl_b32 s83, s83, 6\n"
      "s_add_u32 s84, s84, s83\n"
      "s_lshl_b32 s83, s39, 7\n"
      "s_add_u32 s84, s84, s83\n"
      "v_lshrrev_b32 v50, 2, v4\n"
      "v_mul_u32_u24 v50, 6144, v50\n"
      "v_and_b32 v51, 3, v4\n"
      "v_lshl_add_u32 v50, v51, 4, v50\n"
      "buffer_load_dwordx4 v[68:71], v50, s[88:91], s84 offen\n"
      "s_add_u32 s84, s84, 393216\n"
      "buffer_load_dwordx4 v[72:75], v50, s[88:91], s84 offen\n"
      "s_add_u32 s84, s84, 393216\n"
      "buffer_load_dwordx4 v[76:79], v50, s[88:91], s84 offen\n"
      "s_add_u32 s84, s84, 393216\n"
      "buffer_load_dwordx4 v[80:83], v50, s[88:91], s84 offen\n"
      "s_add_u32 s84, s84, 393216\n"
      "buffer_load_dwordx4 v[84:87], v50, s[88:91], s84 offen\n"
      "s_add_u32 s84, s84, 393216\n"
      "buffer_load_dwordx4 v[88:91], v50, s[88:91], s84 offen\n"
      "s_add_u32 s84, s84, 393216\n"
      "buffer_load_dwordx4 v[92:95], v50, s[88:91], s84 offen\n"
      "s_add_u32 s84, s84, 393216\n"
      "buffer_load_dwordx4 v[96:99], v50, s[88:91], s84 offen\n"
      "s_nop 0\n"
      "Lg0_%=:\n"
      "v_lshrrev_b32 v50, 1, v49\n"
      "v_lshlrev_b32 v50, 8, v50\n"
      "v_lshl_add_u32 v50, v48, 4, v50\n"
      "v_and_b32 v51, 1, v49\n"
      "v_lshl_add_u32 v6, v51, 10, v50\n"
      "v_and_b32 v50, 3, v48\n"
      "v_xor_b32 v50, v50, v49\n"
      "v_and_b32 v51, 12, v48\n"
      "v_xor_b32 v57, 0, v51\n"
      "v_or_b32 v57, v57, v50\n"
      "v_lshlrev_b32 v57, 4, v57\n"
      "v_lshl_add_u32 v57, v48, 10, v57\n"
      "v_add_u32 v101, 65536, v57\n"
      "v_xor_b32 v58, 4, v51\n"
      "v_or_b32 v58, v58, v50\n"
      "v_lshlrev_b32 v58, 4, v58\n"
      "v_lshl_add_u32 v58, v48, 10, v58\n"
      "v_add_u32 v102, 65536, v58\n"
      "v_xor_b32 v59, 8, v51\n"
      "v_or_b32 v59, v59, v50\n"
      "v_lshlrev_b32 v59, 4, v59\n"
      "v_lshl_add_u32 v59, v48, 10, v59\n"
      "v_add_u32 v103, 65536, v59\n"
      "v_xor_b32 v100, 12, v51\n"
      "v_or_b32 v100, v100, v50\n"
      "v_lshlrev_b32 v100, 4, v100\n"
      "v_lshl_add_u32 v100, v48, 10, v100\n"
      "v_add_u32 v104, 65536, v100\n"
      "v_lshlrev_b32 v50, 2, v49\n"
      "v_sub_u32 v50, v48, v50\n"
      "s_cmp_eq_u32 s37, 0\n"
      "s_cselect_b32 s83, 0xf149f2ca, 0\n"
      "v_mov_b32 v105, 0xf149f2ca\n"
      "v_subrev_u32 v51, 0, v50\n"
      "v_cmp_ge_u32 vcc, 7, v51\n"
      "s_nop 1\n"
      "v_cndmask_b32 v108, v105, 0, vcc\n"
      "v_add_u32 v51, 16, v51\n"
      "v_cmp_ge_u32 vcc, 7, v51\n"
      "s_nop 1\n"
      "v_cndmask_b32 v112, v105, 0, vcc\n"
      "v_min_f32 v116, s83, v112\n"
      "v_subrev_u32 v51, 1, v50\n"
      "v_cmp_ge_u32 vcc, 7, v51\n"
      "s_nop 1\n"
      "v_cndmask_b32 v109, v105, 0, vcc\n"
      "v_add_u32 v51, 16, v51\n"
      "v_cmp_ge_u32 vcc, 7, v51\n"
      "s_nop 1\n"
      "v_cndmask_b32 v113, v105, 0, vcc\n"
      "v_min_f32 v117, s83, v113\n"
      "v_subrev_u32 v51, 2, v50\n"
      "v_cmp_ge_u32 vcc, 7, v51\n"
      "s_nop 1\n"
      "v_cndmask_b32 v110, v105, 0, vcc\n"
      "v_add_u32 v51, 16, v51\n"
      "v_cmp_ge_u32 vcc, 7, v51\n"
      "s_nop 1\n"
      "v_cndmask_b32 v114, v105, 0, vcc\n"
      "v_min_f32 v118, s83, v114\n"
      "v_subrev_u32 v51, 3, v50\n"
      "v_cmp_ge_u32 vcc, 7, v51\n"
      "s_nop 1\n"
      "v_cndmask_b32 v111, v105, 0, vcc\n"
      "v_add_u32 v51, 16, v51\n"
      "v_cmp_ge_u32 vcc, 7, v51\n"
      "s_nop 1\n"
      "v_cndmask_b32 v115, v105, 0, vcc\n"
      "v_min_f32 v119, s83, v115\n"
      "v_cmp_gt_u32 vcc, 2, v49\n"
      "v_mov_b32 v50, 0x3f803f80\n"
      "s_nop 0\n"
      "v_cndmask_b32 v128, 0, v50, vcc\n"
      "v_cndmask_b32 v132, v50, 0, vcc\n"
      "v_mov_b32 v136, v50\n"
      "v_cndmask_b32 v129, 0, v50, vcc\n"
      "v_cndmask_b32 v133, v50, 0, vcc\n"
      "v_mov_b32 v137, v50\n"
      "v_cndmask_b32 v130, 0, v50, vcc\n"
      "v_cndmask_b32 v134, v50, 0, vcc\n"
      "v_mov_b32 v138, v50\n"
      "v_cndmask_b32 v131, 0, v50, vcc\n"
      "v_cndmask_b32 v135, v50, 0, vcc\n"
      "v_mov_b32 v139, v50\n"
      "v_lshrrev_b32 v50, 2, v48\n"
      "v_lshl_add_u32 v106, v49, 3, v50\n"
      "v_and_b32 v107, 3, v7\n"
      "v_lshlrev_b32 v107, 3, v107\n"
      "s_waitcnt vmcnt(22)\n"
      "v_lshlrev_b32 v140, 16, v55\n"
      "v_and_b32 v141, 0xffff0000, v55\n"
      "v_mul_f32 v142, 0xbfb8aa3b, v140\n"
      "v_mul_f32 v143, 0xbfb8aa3b, v141\n"
      "v_exp_f32 v142, v142\n"
      "v_exp_f32 v143, v143\n"
      "v_add_f32 v142, 1.0, v142\n"
      "v_add_f32 v143, 1.0, v143\n"
      "v_rcp_f32 v142, v142\n"
      "v_rcp_f32 v143, v143\n"
      "v_mul_f32 v140, v140, v142\n"
      "v_mul_f32 v141, v141, v143\n"
      "v_cvt_pk_bf16_f32 v55, v140, v141\n"
      "v_lshlrev_b32 v50, 2, v4\n"
      "v_add_u32 v50, 161280, v50\n"
      "ds_write_b32 v50, v55\n"
      "s_waitcnt vmcnt(2)\n"
      "v_readfirstlane_b32 s31, v56\n"
      "s_nop 1\n"
      "s_nop 2\n"
      "s_add_u32 s31, s31, 1\n"
      "s_waitcnt lgkmcnt(0)\n"
      "s_barrier\n"
      "s_mov_b32 s92, s64\n"
      "s_and_b32 s93, s65, 0xffff\n"
      "s_mov_b32 s94, 0x200000\n"
      "s_mov_b32 s95, 0x20000\n"
      "s_add_u32 s83, s30, 0\n"
      "s_lshl_b32 s84, s37, 6\n"
      "s_add_u32 s84, s84, s83\n"
      "s_sub_i32 s84, s84, 16\n"
      "s_max_i32 s84, s84, 0\n"
      "s_lshl_b32 s84, s84, 1\n"
      "s_add_u32 s84, s84, s38\n"
      "s_lshl_b32 s87, s84, 10\n"
      "s_lshl_b32 m0, s83, 10\n"
      "s_and_b32 s83, s83, 15\n"
      "v_xor_b32 v105, s83, v7\n"
      "v_lshlrev_b32 v105, 4, v105\n"
      "buffer_load_dwordx4 v105, s[92:95], s87 offen lds\n"
      "s_add_u32 s83, s30, 4\n"
      "s_lshl_b32 s84, s37, 6\n"
      "s_add_u32 s84, s84, s83\n"
      "s_sub_i32 s84, s84, 16\n"
      "s_max_i32 s84, s84, 0\n"
      "s_lshl_b32 s84, s84, 1\n"
      "s_add_u32 s84, s84, s38\n"
      "s_lshl_b32 s87, s84, 10\n"
      "s_lshl_b32 m0, s83, 10\n"
      "s_and_b32 s83, s83, 15\n"
      "v_xor_b32 v105, s83, v7\n"
      "v_lshlrev_b32 v105, 4, v105\n"
      "buffer_load_dwordx4 v105, s[92:95], s87 offen lds\n"
      "s_add_u32 s83, s30, 8\n"
      "s_lshl_b32 s84, s37, 6\n"
      "s_add_u32 s84, s84, s83\n"
      "s_sub_i32 s84, s84, 16\n"
      "s_max_i32 s84, s84, 0\n"
      "s_lshl_b32 s84, s84, 1\n"
      "s_add_u32 s84, s84, s38\n"
      "s_lshl_b32 s87, s84, 10\n"
      "s_lshl_b32 m0, s83, 10\n"
      "s_and_b32 s83, s83, 15\n"
      "v_xor_b32 v105, s83, v7\n"
      "v_lshlrev_b32 v105, 4, v105\n"
      "buffer_load_dwordx4 v105, s[92:95], s87 offen lds\n"
      "s_add_u32 s83, s30, 12\n"
      "s_lshl_b32 s84, s37, 6\n"
      "s_add_u32 s84, s84, s83\n"
      "s_sub_i32 s84, s84, 16\n"
      "s_max_i32 s84, s84, 0\n"
      "s_lshl_b32 s84, s84, 1\n"
      "s_add_u32 s84, s84, s38\n"
      "s_lshl_b32 s87, s84, 10\n"
      "s_lshl_b32 m0, s83, 10\n"
      "s_and_b32 s83, s83, 15\n"
      "v_xor_b32 v105, s83, v7\n"
      "v_lshlrev_b32 v105, 4, v105\n"
      "buffer_load_dwordx4 v105, s[92:95], s87 offen lds\n"
      "s_add_u32 s83, s30, 16\n"
      "s_lshl_b32 s84, s37, 6\n"
      "s_add_u32 s84, s84, s83\n"
      "s_sub_i32 s84, s84, 16\n"
      "s_max_i32 s84, s84, 0\n"
      "s_lshl_b32 s84, s84, 1\n"
      "s_add_u32 s84, s84, s38\n"
      "s_lshl_b32 s87, s84, 10\n"
      "s_lshl_b32 m0, s83, 10\n"
      "s_and_b32 s83, s83, 15\n"
      "v_xor_b32 v105, s83, v7\n"
      "v_lshlrev_b32 v105, 4, v105\n"
      "buffer_load_dwordx4 v105, s[92:95], s87 offen lds\n"
      "s_add_u32 s83, s30, 20\n"
      "s_lshl_b32 s84, s37, 6\n"
      "s_add_u32 s84, s84, s83\n"
      "s_sub_i32 s84, s84, 16\n"
      "s_max_i32 s84, s84, 0\n"
      "s_lshl_b32 s84, s84, 1\n"
      "s_add_u32 s84, s84, s38\n"
      "s_lshl_b32 s87, s84, 10\n"
      "s_lshl_b32 m0, s83, 10\n"
      "s_and_b32 s83, s83, 15\n"
      "v_xor_b32 v105, s83, v7\n"
      "v_lshlrev_b32 v105, 4, v105\n"
      "buffer_load_dwordx4 v105, s[92:95], s87 offen lds\n"
      "s_add_u32 s83, s30, 24\n"
      "s_lshl_b32 s84, s37, 6\n"
      "s_add_u32 s84, s84, s83\n"
      "s_sub_i32 s84, s84, 16\n"
      "s_max_i32 s84, s84, 0\n"
      "s_lshl_b32 s84, s84, 1\n"
      "s_add_u32 s84, s84, s38\n"
      "s_lshl_b32 s87, s84, 10\n"
      "s_lshl_b32 m0, s83, 10\n"
      "s_and_b32 s83, s83, 15\n"
      "v_xor_b32 v105, s83, v7\n"
      "v_lshlrev_b32 v105, 4, v105\n"
      "buffer_load_dwordx4 v105, s[92:95], s87 offen lds\n"
      "s_add_u32 s83, s30, 28\n"
      "s_lshl_b32 s84, s37, 6\n"
      "s_add_u32 s84, s84, s83\n"
      "s_sub_i32 s84, s84, 16\n"
      "s_max_i32 s84, s84, 0\n"
      "s_lshl_b32 s84, s84, 1\n"
      "s_add_u32 s84, s84, s38\n"
      "s_lshl_b32 s87, s84, 10\n"
      "s_lshl_b32 m0, s83, 10\n"
      "s_and_b32 s83, s83, 15\n"
      "v_xor_b32 v105, s83, v7\n"
      "v_lshlrev_b32 v105, 4, v105\n"
      "buffer_load_dwordx4 v105, s[92:95], s87 offen lds\n"
      "s_add_u32 s83, s30, 32\n"
      "s_lshl_b32 s84, s37, 6\n"
      "s_add_u32 s84, s84, s83\n"
      "s_sub_i32 s84, s84, 16\n"
      "s_max_i32 s84, s84, 0\n"
      "s_lshl_b32 s84, s84, 1\n"
      "s_add_u32 s84, s84, s38\n"
      "s_lshl_b32 s87, s84, 10\n"
      "s_lshl_b32 m0, s83, 10\n"
      "s_and_b32 s83, s83, 15\n"
      "v_xor_b32 v105, s83, v7\n"
      "v_lshlrev_b32 v105, 4, v105\n"
      "buffer_load_dwordx4 v105, s[92:95], s87 offen lds\n"
      "s_add_u32 s83, s30, 36\n"
      "s_lshl_b32 s84, s37, 6\n"
      "s_add_u32 s84, s84, s83\n"
      "s_sub_i32 s84, s84, 16\n"
      "s_max_i32 s84, s84, 0\n"
      "s_lshl_b32 s84, s84, 1\n"
      "s_add_u32 s84, s84, s38\n"
      "s_lshl_b32 s87, s84, 10\n"
      "s_lshl_b32 m0, s83, 10\n"
      "s_and_b32 s83, s83, 15\n"
      "v_xor_b32 v105, s83, v7\n"
      "v_lshlrev_b32 v105, 4, v105\n"
      "buffer_load_dwordx4 v105, s[92:95], s87 offen lds\n"
      "s_add_u32 s83, s30, 40\n"
      "s_lshl_b32 s84, s37, 6\n"
      "s_add_u32 s84, s84, s83\n"
      "s_sub_i32 s84, s84, 16\n"
      "s_max_i32 s84, s84, 0\n"
      "s_lshl_b32 s84, s84, 1\n"
      "s_add_u32 s84, s84, s38\n"
      "s_lshl_b32 s87, s84, 10\n"
      "s_lshl_b32 m0, s83, 10\n"
      "s_and_b32 s83, s83, 15\n"
      "v_xor_b32 v105, s83, v7\n"
      "v_lshlrev_b32 v105, 4, v105\n"
      "buffer_load_dwordx4 v105, s[92:95], s87 offen lds\n"
      "s_add_u32 s83, s30, 44\n"
      "s_lshl_b32 s84, s37, 6\n"
      "s_add_u32 s84, s84, s83\n"
      "s_sub_i32 s84, s84, 16\n"
      "s_max_i32 s84, s84, 0\n"
      "s_lshl_b32 s84, s84, 1\n"
      "s_add_u32 s84, s84, s38\n"
      "s_lshl_b32 s87, s84, 10\n"
      "s_lshl_b32 m0, s83, 10\n"
      "s_and_b32 s83, s83, 15\n"
      "v_xor_b32 v105, s83, v7\n"
      "v_lshlrev_b32 v105, 4, v105\n"
      "buffer_load_dwordx4 v105, s[92:95], s87 offen lds\n"
      "s_add_u32 s83, s30, 48\n"
      "s_lshl_b32 s84, s37, 6\n"
      "s_add_u32 s84, s84, s83\n"
      "s_sub_i32 s84, s84, 16\n"
      "s_max_i32 s84, s84, 0\n"
      "s_lshl_b32 s84, s84, 1\n"
      "s_add_u32 s84, s84, s38\n"
      "s_lshl_b32 s87, s84, 10\n"
      "s_lshl_b32 m0, s83, 10\n"
      "s_and_b32 s83, s83, 15\n"
      "v_xor_b32 v105, s83, v7\n"
      "v_lshlrev_b32 v105, 4, v105\n"
      "buffer_load_dwordx4 v105, s[92:95], s87 offen lds\n"
      "s_add_u32 s83, s30, 52\n"
      "s_lshl_b32 s84, s37, 6\n"
      "s_add_u32 s84, s84, s83\n"
      "s_sub_i32 s84, s84, 16\n"
      "s_max_i32 s84, s84, 0\n"
      "s_lshl_b32 s84, s84, 1\n"
      "s_add_u32 s84, s84, s38\n"
      "s_lshl_b32 s87, s84, 10\n"
      "s_lshl_b32 m0, s83, 10\n"
      "s_and_b32 s83, s83, 15\n"
      "v_xor_b32 v105, s83, v7\n"
      "v_lshlrev_b32 v105, 4, v105\n"
      "buffer_load_dwordx4 v105, s[92:95], s87 offen lds\n"
      "s_add_u32 s83, s30, 56\n"
      "s_lshl_b32 s84, s37, 6\n"
      "s_add_u32 s84, s84, s83\n"
      "s_sub_i32 s84, s84, 16\n"
      "s_max_i32 s84, s84, 0\n"
      "s_lshl_b32 s84, s84, 1\n"
      "s_add_u32 s84, s84, s38\n"
      "s_lshl_b32 s87, s84, 10\n"
      "s_lshl_b32 m0, s83, 10\n"
      "s_and_b32 s83, s83, 15\n"
      "v_xor_b32 v105, s83, v7\n"
      "v_lshlrev_b32 v105, 4, v105\n"
      "buffer_load_dwordx4 v105, s[92:95], s87 offen lds\n"
      "s_add_u32 s83, s30, 60\n"
      "s_lshl_b32 s84, s37, 6\n"
      "s_add_u32 s84, s84, s83\n"
      "s_sub_i32 s84, s84, 16\n"
      "s_max_i32 s84, s84, 0\n"
      "s_lshl_b32 s84, s84, 1\n"
      "s_add_u32 s84, s84, s38\n"
      "s_lshl_b32 s87, s84, 10\n"
      "s_lshl_b32 m0, s83, 10\n"
      "s_and_b32 s83, s83, 15\n"
      "v_xor_b32 v105, s83, v7\n"
      "v_lshlrev_b32 v105, 4, v105\n"
      "buffer_load_dwordx4 v105, s[92:95], s87 offen lds\n"
      "s_add_u32 s83, s30, 64\n"
      "s_lshl_b32 s84, s37, 6\n"
      "s_add_u32 s84, s84, s83\n"
      "s_sub_i32 s84, s84, 16\n"
      "s_max_i32 s84, s84, 0\n"
      "s_lshl_b32 s84, s84, 1\n"
      "s_add_u32 s84, s84, s38\n"
      "s_lshl_b32 s87, s84, 10\n"
      "s_lshl_b32 m0, s83, 10\n"
      "s_and_b32 s83, s83, 15\n"
      "v_xor_b32 v105, s83, v7\n"
      "v_lshlrev_b32 v105, 4, v105\n"
      "buffer_load_dwordx4 v105, s[92:95], s87 offen lds\n"
      "s_add_u32 s83, s30, 68\n"
      "s_lshl_b32 s84, s37, 6\n"
      "s_add_u32 s84, s84, s83\n"
      "s_sub_i32 s84, s84, 16\n"
      "s_max_i32 s84, s84, 0\n"
      "s_lshl_b32 s84, s84, 1\n"
      "s_add_u32 s84, s84, s38\n"
      "s_lshl_b32 s87, s84, 10\n"
      "s_lshl_b32 m0, s83, 10\n"
      "s_and_b32 s83, s83, 15\n"
      "v_xor_b32 v105, s83, v7\n"
      "v_lshlrev_b32 v105, 4, v105\n"
      "buffer_load_dwordx4 v105, s[92:95], s87 offen lds\n"
      "s_add_u32 s83, s30, 72\n"
      "s_lshl_b32 s84, s37, 6\n"
      "s_add_u32 s84, s84, s83\n"
      "s_sub_i32 s84, s84, 16\n"
      "s_max_i32 s84, s84, 0\n"
      "s_lshl_b32 s84, s84, 1\n"
      "s_add_u32 s84, s84, s38\n"
      "s_lshl_b32 s87, s84, 10\n"
      "s_lshl_b32 m0, s83, 10\n"
      "s_and_b32 s83, s83, 15\n"
      "v_xor_b32 v105, s83, v7\n"
      "v_lshlrev_b32 v105, 4, v105\n"
      "buffer_load_dwordx4 v105, s[92:95], s87 offen lds\n"
      "s_add_u32 s83, s30, 76\n"
      "s_lshl_b32 s84, s37, 6\n"
      "s_add_u32 s84, s84, s83\n"
      "s_sub_i32 s84, s84, 16\n"
      "s_max_i32 s84, s84, 0\n"
      "s_lshl_b32 s84, s84, 1\n"
      "s_add_u32 s84, s84, s38\n"
      "s_lshl_b32 s87, s84, 10\n"
      "s_lshl_b32 m0, s83, 10\n"
      "s_and_b32 s83, s83, 15\n"
      "v_xor_b32 v105, s83, v7\n"
      "v_lshlrev_b32 v105, 4, v105\n"
      "buffer_load_dwordx4 v105, s[92:95], s87 offen lds\n"
      "v_lshlrev_b32 v55, 4, v49\n"
      "v_add_u32 v55, 161280, v55\n"
      "v_bfe_u32 v50, v48, 3, 1\n"
      "v_and_b32 v51, 1, v49\n"
      "v_lshl_or_b32 v50, v51, 1, v50\n"
      "v_xor_b32 v50, s30, v50\n"
      "v_lshlrev_b32 v50, 5, v50\n"
      "v_lshl_add_u32 v56, v106, 7, v50\n"
      "v_add_u32 v56, v56, v107\n"
      "v_add_u32 v56, 81920, v56\n"
      "ds_read_b128 v[140:143], v55\n"
      "ds_read_b64_tr_b16 v[156:157], v56\n"
      "ds_read_b64_tr_b16 v[158:159], v56 offset:512\n"
      "ds_read_b128 v[144:147], v55 offset:64\n"
      "ds_read_b64_tr_b16 v[160:161], v56 offset:4096\n"
      "ds_read_b64_tr_b16 v[162:163], v56 offset:4608\n"
      "ds_read_b128 v[148:151], v55 offset:128\n"
      "ds_read_b64_tr_b16 v[164:165], v56 offset:8192\n"
      "ds_read_b64_tr_b16 v[166:167], v56 offset:8704\n"
      "ds_read_b128 v[152:155], v55 offset:192\n"
      "ds_read_b64_tr_b16 v[168:169], v56 offset:12288\n"
      "ds_read_b64_tr_b16 v[170:171], v56 offset:12800\n"
      "s_waitcnt lgkmcnt(9)\n"
      "v_mfma_f32_16x16x32_bf16 v[172:175], v[140:143], v[156:159], 0\n"
      "ds_read_b128 v[140:143], v55 offset:256\n"
      "ds_read_b64_tr_b16 v[156:157], v56 offset:16384\n"
      "ds_read_b64_tr_b16 v[158:159], v56 offset:16896\n"
      "s_waitcnt lgkmcnt(9)\n"
      "v_mfma_f32_16x16x32_bf16 v[172:175], v[144:147], v[160:163], v[172:175]\n"
      "ds_read_b128 v[144:147], v55 offset:320\n"
      "ds_read_b64_tr_b16 v[160:161], v56 offset:20480\n"
      "ds_read_b64_tr_b16 v[162:163], v56 offset:20992\n"
      "s_waitcnt lgkmcnt(9)\n"
      "v_mfma_f32_16x16x32_bf16 v[172:175], v[148:151], v[164:167], v[172:175]\n"
      "ds_read_b128 v[148:151], v55 offset:384\n"
      "ds_read_b64_tr_b16 v[164:165], v56 offset:24576\n"
      "ds_read_b64_tr_b16 v[166:167], v56 offset:25088\n"
      "s_waitcnt lgkmcnt(9)\n"
      "v_mfma_f32_16x16x32_bf16 v[172:175], v[152:155], v[168:171], v[172:175]\n"
      "ds_read_b128 v[152:155], v55 offset:448\n"
      "ds_read_b64_tr_b16 v[168:169], v56 offset:28672\n"
      "ds_read_b64_tr_b16 v[170:171], v56 offset:29184\n"
      "s_waitcnt lgkmcnt(9)\n"
      "v_mfma_f32_16x16x32_bf16 v[172:175], v[140:143], v[156:159], v[172:175]\n"
      "ds_read_b128 v[140:143], v55 offset:512\n"
      "ds_read_b64_tr_b16 v[156:157], v56 offset:32768\n"
      "ds_read_b64_tr_b16 v[158:159], v56 offset:33280\n"
      "s_waitcnt lgkmcnt(9)\n"
      "v_mfma_f32_16x16x32_bf16 v[172:175], v[144:147], v[160:163], v[172:175]\n"
      "ds_read_b128 v[144:147], v55 offset:576\n"
      "ds_read_b64_tr_b16 v[160:161], v56 offset:36864\n"
      "ds_read_b64_tr_b16 v[162:163], v56 offset:37376\n"
      "s_waitcnt lgkmcnt(9)\n"
      "v_mfma_f32_16x16x32_bf16 v[172:175], v[148:151], v[164:167], v[172:175]\n"
      "ds_read_b128 v[148:151], v55 offset:640\n"
      "ds_read_b64_tr_b16 v[164:165], v56 offset:40960\n"
      "ds_read_b64_tr_b16 v[166:167], v56 offset:41472\n"
      "s_waitcnt lgkmcnt(9)\n"
      "v_mfma_f32_16x16x32_bf16 v[172:175], v[152:155], v[168:171], v[172:175]\n"
      "ds_read_b128 v[152:155], v55 offset:704\n"
      "ds_read_b64_tr_b16 v[168:169], v56 offset:45056\n"
      "ds_read_b64_tr_b16 v[170:171], v56 offset:45568\n"
      "s_waitcnt lgkmcnt(9)\n"
      "v_mfma_f32_16x16x32_bf16 v[172:175], v[140:143], v[156:159], v[172:175]\n"
      "ds_read_b128 v[140:143], v55 offset:768\n"
      "ds_read_b64_tr_b16 v[156:157], v56 offset:49152\n"
      "ds_read_b64_tr_b16 v[158:159], v56 offset:49664\n"
      "s_waitcnt lgkmcnt(9)\n"
      "v_mfma_f32_16x16x32_bf16 v[172:175], v[144:147], v[160:163], v[172:175]\n"
      "ds_read_b128 v[144:147], v55 offset:832\n"
      "ds_read_b64_tr_b16 v[160:161], v56 offset:53248\n"
      "ds_read_b64_tr_b16 v[162:163], v56 offset:53760\n"
      "s_waitcnt lgkmcnt(9)\n"
      "v_mfma_f32_16x16x32_bf16 v[172:175], v[148:151], v[164:167], v[172:175]\n"
      "ds_read_b128 v[148:151], v55 offset:896\n"
      "ds_read_b64_tr_b16 v[164:165], v56 offset:57344\n"
      "ds_read_b64_tr_b16 v[166:167], v56 offset:57856\n"
      "s_waitcnt lgkmcnt(9)\n"
      "v_mfma_f32_16x16x32_bf16 v[172:175], v[152:155], v[168:171], v[172:175]\n"
      "ds_read_b128 v[152:155], v55 offset:960\n"
      "ds_read_b64_tr_b16 v[168:169], v56 offset:61440\n"
      "ds_read_b64_tr_b16 v[170:171], v56 offset:61952\n"
      "s_waitcnt lgkmcnt(9)\n"
      "v_mfma_f32_16x16x32_bf16 v[172:175], v[140:143], v[156:159], v[172:175]\n"
      "s_waitcnt lgkmcnt(6)\n"
      "v_mfma_f32_16x16x32_bf16 v[172:175], v[144:147], v[160:163], v[172:175]\n"
      "s_waitcnt lgkmcnt(3)\n"
      "v_mfma_f32_16x16x32_bf16 v[172:175], v[148:151], v[164:167], v[172:175]\n"
      "s_waitcnt lgkmcnt(0)\n"
      "v_mfma_f32_16x16x32_bf16 v[172:175], v[152:155], v[168:171], v[172:175]\n"
      "s_nop 7\n"
      "v_cvt_pk_bf16_f32 v50, v172, v172\n"
      "v_lshlrev_b32 v50, 16, v50\n"
      "v_add_f32 v51, 1.0, v50\n"
      "v_cvt_pk_bf16_f32 v51, v51, v51\n"
      "v_lshlrev_b32 v51, 16, v51\n"
      "s_cmp_ge_u32 s30, 2\n"
      "s_cselect_b64 vcc, -1, 0\n"
      "s_nop 0\n"
      "v_cndmask_b32 v50, v50, v51, vcc\n"
      "s_lshl_b32 s83, s30, 6\n"
      "s_add_u32 s83, s83, 162304\n"
      "v_lshlrev_b32 v51, 2, v48\n"
      "v_add_u32 v51, s83, v51\n"
      "ds_write_b32 v51, v50\n"
      "s_waitcnt lgkmcnt(0)\n"
      "s_barrier\n"
      "v_lshlrev_b32 v50, 5, v49\n"
      "v_add_u32 v50, 162304, v50\n"
      "ds_read_b128 v[140:143], v50\n"
      "ds_read_b128 v[144:147], v50 offset:16\n"
      "ds_read_b128 v[148:151], v50 offset:128\n"
      "ds_read_b128 v[152:155], v50 offset:144\n"
      "s_cmp_eq_u32 s35, 0\n"
      "s_mov_b32 s83, 96\n"
      "s_cselect_b32 s83, 32, s83\n"
      "s_mov_b32 s84, 32\n"
      "s_cselect_b32 s84, 128, s84\n"
      "s_mul_i32 s83, s83, s30\n"
      "s_add_u32 s83, s83, 147456\n"
      "v_mul_u32_u24 v51, 384, v106\n"
      "v_add_u32 v51, v51, v107\n"
      "v_add_u32 v51, s83, v51\n"
      "ds_read_b64_tr_b16 v[156:157], v51\n"
      "ds_read_b64_tr_b16 v[158:159], v51 offset:1536\n"
      "v_add_u32 v51, s84, v51\n"
      "ds_read_b64_tr_b16 v[160:161], v51\n"
      "ds_read_b64_tr_b16 v[162:163], v51 offset:1536\n"
      "v_add_u32 v51, s84, v51\n"
      "ds_read_b64_tr_b16 v[164:165], v51\n"
      "ds_read_b64_tr_b16 v[166:167], v51 offset:1536\n"
      "s_waitcnt lgkmcnt(6)\n"
      "v_cvt_pk_bf16_f32 v168, v140, v141\n"
      "v_cvt_pk_bf16_f32 v169, v142, v143\n"
      "v_cvt_pk_bf16_f32 v170, v144, v145\n"
      "v_cvt_pk_bf16_f32 v171, v146, v147\n"
      "s_waitcnt lgkmcnt(0)\n"
      "s_lshl_b32 s83, s39, 2\n"
      "s_add_u32 s83, s83, s30\n"
      "s_mul_i32 s83, s83, 164864\n"
      "s_mul_i32 s84, s35, 68608\n"
      "s_add_u32 s98, s83, s84\n"
      "s_mul_i32 s84, s36, 3072\n"
      "s_add_u32 s99, s98, s84\n"
      "s_add_u32 s99, s99, 3072\n"
      "v_mfma_f32_16x16x32_bf16 v[172:175], v[168:171], v[156:159], 0\n"
      "v_lshlrev_b32 v208, 16, v156\n"
      "v_and_b32 v209, 0xffff0000, v156\n"
      "v_lshlrev_b32 v210, 16, v157\n"
      "v_and_b32 v211, 0xffff0000, v157\n"
      "v_lshlrev_b32 v212, 16, v158\n"
      "v_and_b32 v213, 0xffff0000, v158\n"
      "v_lshlrev_b32 v214, 16, v159\n"
      "v_and_b32 v215, 0xffff0000, v159\n"
      "v_pk_mul_f32 v[208:209], v[208:209], v[148:149] op_sel:[0,0] op_sel_hi:[1,1]\n"
      "v_pk_mul_f32 v[210:211], v[210:211], v[150:151] op_sel:[0,0] op_sel_hi:[1,1]\n"
      "v_pk_mul_f32 v[212:213], v[212:213], v[152:153] op_sel:[0,0] op_sel_hi:[1,1]\n"
      "v_pk_mul_f32 v[214:215], v[214:215], v[154:155] op_sel:[0,0] op_sel_hi:[1,1]\n"
      "v_cvt_pk_bf16_f32 v196, v208, v209\n"
      "v_cvt_pk_bf16_f32 v197, v210, v211\n"
      "v_cvt_pk_bf16_f32 v198, v212, v213\n"
      "v_cvt_pk_bf16_f32 v199, v214, v215\n"
      "s_nop 1\n"
      "v_mfma_f32_16x16x32_bf16 v[184:187], v[136:139], v[196:199], 0\n"
      "buffer_store_dwordx4 v[196:199], v5, s[20:23], s99 offen sc1\n"
      "v_mfma_f32_16x16x32_bf16 v[176:179], v[168:171], v[160:163], 0\n"
      "v_lshlrev_b32 v208, 16, v160\n"
      "v_and_b32 v209, 0xffff0000, v160\n"
      "v_lshlrev_b32 v210, 16, v161\n"
      "v_and_b32 v211, 0xffff0000, v161\n"
      "v_lshlrev_b32 v212, 16, v162\n"
      "v_and_b32 v213, 0xffff0000, v162\n"
      "v_lshlrev_b32 v214, 16, v163\n"
      "v_and_b32 v215, 0xffff0000, v163\n"
      "v_pk_mul_f32 v[208:209], v[208:209], v[148:149] op_sel:[0,0] op_sel_hi:[1,1]\n"
      "v_pk_mul_f32 v[210:211], v[210:211], v[150:151] op_sel:[0,0] op_sel_hi:[1,1]\n"
      "v_pk_mul_f32 v[212:213], v[212:213], v[152:153] op_sel:[0,0] op_sel_hi:[1,1]\n"
      "v_pk_mul_f32 v[214:215], v[214:215], v[154:155] op_sel:[0,0] op_sel_hi:[1,1]\n"
      "v_cvt_pk_bf16_f32 v200, v208, v209\n"
      "v_cvt_pk_bf16_f32 v201, v210, v211\n"
      "v_cvt_pk_bf16_f32 v202, v212, v213\n"
      "v_cvt_pk_bf16_f32 v203, v214, v215\n"
      "s_nop 1\n"
      "v_mfma_f32_16x16x32_bf16 v[188:191], v[136:139], v[200:203], 0\n"
      "buffer_store_dwordx4 v[200:203], v5, s[20:23], s99 offen offset:1024 sc1\n"
      "v_mfma_f32_16x16x32_bf16 v[180:183], v[168:171], v[164:167], 0\n"
      "v_lshlrev_b32 v208, 16, v164\n"
      "v_and_b32 v209, 0xffff0000, v164\n"
      "v_lshlrev_b32 v210, 16, v165\n"
      "v_and_b32 v211, 0xffff0000, v165\n"
      "v_lshlrev_b32 v212, 16, v166\n"
      "v_and_b32 v213, 0xffff0000, v166\n"
      "v_lshlrev_b32 v214, 16, v167\n"
      "v_and_b32 v215, 0xffff0000, v167\n"
      "v_pk_mul_f32 v[208:209], v[208:209], v[148:149] op_sel:[0,0] op_sel_hi:[1,1]\n"
      "v_pk_mul_f32 v[210:211], v[210:211], v[150:151] op_sel:[0,0] op_sel_hi:[1,1]\n"
      "v_pk_mul_f32 v[212:213], v[212:213], v[152:153] op_sel:[0,0] op_sel_hi:[1,1]\n"
      "v_pk_mul_f32 v[214:215], v[214:215], v[154:155] op_sel:[0,0] op_sel_hi:[1,1]\n"
      "v_cvt_pk_bf16_f32 v204, v208, v209\n"
      "v_cvt_pk_bf16_f32 v205, v210, v211\n"
      "v_cvt_pk_bf16_f32 v206, v212, v213\n"
      "v_cvt_pk_bf16_f32 v207, v214, v215\n"
      "s_nop 1\n"
      "v_mfma_f32_16x16x32_bf16 v[192:195], v[136:139], v[204:207], 0\n"
      "buffer_store_dwordx4 v[204:207], v5, s[20:23], s99 offen offset:2048 sc1\n"
      "s_lshr_b32 s83, s36, 3\n"
      "s_lshl_b32 s83, s83, 8\n"
      "s_and_b32 s84, s36, 7\n"
      "s_lshl_b32 s84, s84, 1\n"
      "s_add_u32 s83, s83, s84\n"
      "s_add_u32 s98, s98, s83\n"
      "v_lshlrev_b32 v51, 4, v48\n"
      "s_mov_b64 exec, 0xffff\n"
      "v_cvt_pk_bf16_f32 v173, v172, v172\n"
      "buffer_store_short v173, v51, s[20:23], s98 offen offset:0 sc1\n"
      "v_cvt_pk_bf16_f32 v185, v184, v184\n"
      "buffer_store_short v185, v51, s[20:23], s98 offen offset:512 sc1\n"
      "v_cvt_pk_bf16_f32 v177, v176, v176\n"
      "buffer_store_short v177, v51, s[20:23], s98 offen offset:1024 sc1\n"
      "v_cvt_pk_bf16_f32 v189, v188, v188\n"
      "buffer_store_short v189, v51, s[20:23], s98 offen offset:1536 sc1\n"
      "v_cvt_pk_bf16_f32 v181, v180, v180\n"
      "buffer_store_short v181, v51, s[20:23], s98 offen offset:2048 sc1\n"
      "v_cvt_pk_bf16_f32 v193, v192, v192\n"
      "buffer_store_short v193, v51, s[20:23], s98 offen offset:2560 sc1\n"
      "s_mov_b64 exec, -1\n"
      "s_waitcnt vmcnt(0)\n"
      "s_barrier\n"
      "s_cmp_lg_u32 s30, 0\n"
      "s_cbranch_scc1 Lfa_%=\n"
      "s_lshl_b32 s83, s39, 7\n"
      "s_lshl_b32 s84, s35, 6\n"
      "s_add_u32 s83, s83, s84\n"
      "s_lshl_b32 s84, s36, 2\n"
      "s_add_u32 s83, s83, s84\n"
      "s_add_u32 s83, s83, 5312512\n"
      "v_mov_b32 v140, 0\n"
      "v_mov_b32 v141, s31\n"
      "s_mov_b64 exec, 1\n"
      "buffer_store_dword v141, v140, s[20:23], s83 offen sc1\n"
      "s_mov_b64 exec, -1\n"
      "s_nop 0\n"
      "Lfa_%=:\n"
      "v_lshlrev_b32 v50, 4, v4\n"
      "v_add_u32 v50, 81920, v50\n"
      "ds_write_b128 v50, v[60:63]\n"
      "ds_write_b128 v50, v[64:67] offset:4096\n"
      "s_lshl_b32 s83, s30, 8\n"
      "v_add_u32 v53, s83, v57\n"
      "v_add_u32 v55, s83, v58\n"
      "v_add_u32 v56, s83, v59\n"
      "v_add_u32 v105, s83, v100\n"
      "ds_read_b128 v[60:63], v53\n"
      "ds_read_b128 v[64:67], v53 offset:16384\n"
      "ds_read_b128 v[140:143], v53 offset:32768\n"
      "ds_read_b128 v[144:147], v53 offset:49152\n"
      "v_add_u32 v212, 65536, v53\n"
      "ds_read_b128 v[148:151], v212\n"
      "ds_read_b128 v[152:155], v55\n"
      "ds_read_b128 v[156:159], v55 offset:16384\n"
      "ds_read_b128 v[160:163], v55 offset:32768\n"
      "ds_read_b128 v[164:167], v55 offset:49152\n"
      "v_add_u32 v212, 65536, v55\n"
      "ds_read_b128 v[168:171], v212\n"
      "s_waitcnt lgkmcnt(5)\n"
      "v_mfma_f32_16x16x32_bf16 v[172:175], v[136:139], v[60:63], 0\n"
      "v_mfma_f32_16x16x32_bf16 v[192:195], v[60:63], v[60:63], 0\n"
      "v_mfma_f32_16x16x32_bf16 v[176:179], v[136:139], v[64:67], 0\n"
      "v_mfma_f32_16x16x32_bf16 v[196:199], v[64:67], v[64:67], 0\n"
      "v_mfma_f32_16x16x32_bf16 v[180:183], v[136:139], v[140:143], 0\n"
      "v_mfma_f32_16x16x32_bf16 v[200:203], v[140:143], v[140:143], 0\n"
      "v_mfma_f32_16x16x32_bf16 v[184:187], v[136:139], v[144:147], 0\n"
      "v_mfma_f32_16x16x32_bf16 v[204:207], v[144:147], v[144:147], 0\n"
      "v_mfma_f32_16x16x32_bf16 v[188:191], v[136:139], v[148:151], 0\n"
      "v_mfma_f32_16x16x32_bf16 v[208:211], v[148:151], v[148:151], 0\n"
      "ds_read_b128 v[60:63], v56\n"
      "ds_read_b128 v[64:67], v56 offset:16384\n"
      "ds_read_b128 v[140:143], v56 offset:32768\n"
      "ds_read_b128 v[144:147], v56 offset:49152\n"
      "v_add_u32 v212, 65536, v56\n"
      "ds_read_b128 v[148:151], v212\n"
      "s_waitcnt lgkmcnt(5)\n"
      "v_mfma_f32_16x16x32_bf16 v[172:175], v[136:139], v[152:155], v[172:175]\n"
      "v_mfma_f32_16x16x32_bf16 v[192:195], v[152:155], v[152:155], v[192:195]\n"
      "v_mfma_f32_16x16x32_bf16 v[176:179], v[136:139], v[156:159], v[176:179]\n"
      "v_mfma_f32_16x16x32_bf16 v[196:199], v[156:159], v[156:159], v[196:199]\n"
      "v_mfma_f32_16x16x32_bf16 v[180:183], v[136:139], v[160:163], v[180:183]\n"
      "v_mfma_f32_16x16x32_bf16 v[200:203], v[160:163], v[160:163], v[200:203]\n"
      "v_mfma_f32_16x16x32_bf16 v[184:187], v[136:139], v[164:167], v[184:187]\n"
      "v_mfma_f32_16x16x32_bf16 v[204:207], v[164:167], v[164:167], v[204:207]\n"
      "v_mfma_f32_16x16x32_bf16 v[188:191], v[136:139], v[168:171], v[188:191]\n"
      "v_mfma_f32_16x16x32_bf16 v[208:211], v[168:171], v[168:171], v[208:211]\n"
      "ds_read_b128 v[152:155], v105\n"
      "ds_read_b128 v[156:159], v105 offset:16384\n"
      "ds_read_b128 v[160:163], v105 offset:32768\n"
      "ds_read_b128 v[164:167], v105 offset:49152\n"
      "v_add_u32 v212, 65536, v105\n"
      "ds_read_b128 v[168:171], v212\n"
      "s_waitcnt lgkmcnt(5)\n"
      "v_mfma_f32_16x16x32_bf16 v[172:175], v[136:139], v[60:63], v[172:175]\n"
      "v_mfma_f32_16x16x32_bf16 v[192:195], v[60:63], v[60:63], v[192:195]\n"
      "v_mfma_f32_16x16x32_bf16 v[176:179], v[136:139], v[64:67], v[176:179]\n"
      "v_mfma_f32_16x16x32_bf16 v[196:199], v[64:67], v[64:67], v[196:199]\n"
      "v_mfma_f32_16x16x32_bf16 v[180:183], v[136:139], v[140:143], v[180:183]\n"
      "v_mfma_f32_16x16x32_bf16 v[200:203], v[140:143], v[140:143], v[200:203]\n"
      "v_mfma_f32_16x16x32_bf16 v[184:187], v[136:139], v[144:147], v[184:187]\n"
      "v_mfma_f32_16x16x32_bf16 v[204:207], v[144:147], v[144:147], v[204:207]\n"
      "v_mfma_f32_16x16x32_bf16 v[188:191], v[136:139], v[148:151], v[188:191]\n"
      "v_mfma_f32_16x16x32_bf16 v[208:211], v[148:151], v[148:151], v[208:211]\n"
      "s_waitcnt lgkmcnt(0)\n"
      "v_mfma_f32_16x16x32_bf16 v[172:175], v[136:139], v[152:155], v[172:175]\n"
      "v_mfma_f32_16x16x32_bf16 v[192:195], v[152:155], v[152:155], v[192:195]\n"
      "v_mfma_f32_16x16x32_bf16 v[176:179], v[136:139], v[156:159], v[176:179]\n"
      "v_mfma_f32_16x16x32_bf16 v[196:199], v[156:159], v[156:159], v[196:199]\n"
      "v_mfma_f32_16x16x32_bf16 v[180:183], v[136:139], v[160:163], v[180:183]\n"
      "v_mfma_f32_16x16x32_bf16 v[200:203], v[160:163], v[160:163], v[200:203]\n"
      "v_mfma_f32_16x16x32_bf16 v[184:187], v[136:139], v[164:167], v[184:187]\n"
      "v_mfma_f32_16x16x32_bf16 v[204:207], v[164:167], v[164:167], v[204:207]\n"
      "v_mfma_f32_16x16x32_bf16 v[188:191], v[136:139], v[168:171], v[188:191]\n"
      "v_mfma_f32_16x16x32_bf16 v[208:211], v[168:171], v[168:171], v[208:211]\n"
      "v_and_b32 v50, 3, v48\n"
      "v_cmp_eq_u32 s[44:45], 1, v50\n"
      "v_cmp_eq_u32 s[46:47], 2, v50\n"
      "v_cmp_eq_u32 s[48:49], 3, v50\n"
      "v_lshrrev_b32 v50, 2, v48\n"
      "v_cmp_eq_u32 vcc, v50, v49\n"
      "s_mul_i32 s83, s30, 640\n"
      "s_add_u32 s83, s83, 147456\n"
      "v_lshlrev_b32 v53, 2, v48\n"
      "v_add_u32 v53, s83, v53\n"
      "v_mov_b32 v51, 150016\n"
      "v_cndmask_b32 v55, v51, v53, vcc\n"
      "v_cndmask_b32 v192, v192, v193, s[44:45]\n"
      "v_cndmask_b32 v192, v192, v194, s[46:47]\n"
      "v_cndmask_b32 v192, v192, v195, s[48:49]\n"
      "v_cndmask_b32 v196, v196, v197, s[44:45]\n"
      "v_cndmask_b32 v196, v196, v198, s[46:47]\n"
      "v_cndmask_b32 v196, v196, v199, s[48:49]\n"
      "v_cndmask_b32 v200, v200, v201, s[44:45]\n"
      "v_cndmask_b32 v200, v200, v202, s[46:47]\n"
      "v_cndmask_b32 v200, v200, v203, s[48:49]\n"
      "v_cndmask_b32 v204, v204, v205, s[44:45]\n"
      "v_cndmask_b32 v204, v204, v206, s[46:47]\n"
      "v_cndmask_b32 v204, v204, v207, s[48:49]\n"
      "v_cndmask_b32 v208, v208, v209, s[44:45]\n"
      "v_cndmask_b32 v208, v208, v210, s[46:47]\n"
      "v_cndmask_b32 v208, v208, v211, s[48:49]\n"
      "ds_write_b32 v53, v172\n"
      "ds_write_b32 v55, v192 offset:320\n"
      "ds_write_b32 v53, v176 offset:64\n"
      "ds_write_b32 v55, v196 offset:384\n"
      "ds_write_b32 v53, v180 offset:128\n"
      "ds_write_b32 v55, v200 offset:448\n"
      "ds_write_b32 v53, v184 offset:192\n"
      "ds_write_b32 v55, v204 offset:512\n"
      "ds_write_b32 v53, v188 offset:256\n"
      "ds_write_b32 v55, v208 offset:576\n"
      "s_waitcnt lgkmcnt(0)\n"
      "s_barrier\n"
      "s_lshl_b32 s83, s30, 5\n"
      "s_add_u32 s83, s83, 81920\n"
      "v_lshl_add_u32 v51, v106, 7, v107\n"
      "v_add_u32 v51, s83, v51\n"
      "ds_read_b64_tr_b16 v[60:61], v51\n"
      "ds_read_b64_tr_b16 v[62:63], v51 offset:512\n"
      "ds_read_b64_tr_b16 v[64:65], v51 offset:4096\n"
      "ds_read_b64_tr_b16 v[66:67], v51 offset:4608\n"
      "v_lshlrev_b32 v50, 2, v4\n"
      "v_min_u32 v50, 316, v50\n"
      "v_add_u32 v50, 147456, v50\n"
      "ds_read_b32 v140, v50\n"
      "ds_read_b32 v141, v50 offset:320\n"
      "ds_read_b32 v142, v50 offset:640\n"
      "ds_read_b32 v143, v50 offset:960\n"
      "ds_read_b32 v144, v50 offset:1280\n"
      "ds_read_b32 v145, v50 offset:1600\n"
      "ds_read_b32 v146, v50 offset:1920\n"
      "ds_read_b32 v147, v50 offset:2240\n"
      "s_waitcnt lgkmcnt(8)\n"
      "s_add_u32 s83, s96, 0\n"
      "s_cmp_lt_u32 s83, 16\n"
      "s_mov_b32 s84, 102\n"
      "s_cselect_b32 s84, 51, s84\n"
      "s_add_u32 s84, s84, s83\n"
      "s_lshl_b32 s85, s39, 2\n"
      "s_add_u32 s85, s85, s30\n"
      "s_mul_i32 s85, s85, 161\n"
      "s_add_u32 s84, s84, s85\n"
      "s_lshl_b32 s84, s84, 10\n"
      "s_cmp_lt_u32 s83, 59\n"
      "s_cselect_b32 s4, s84, 0x7ff00000\n"
      "buffer_store_dwordx4 v[60:63], v5, s[20:23], s4 offen sc1\n"
      "s_add_u32 s83, s96, 32\n"
      "s_cmp_lt_u32 s83, 16\n"
      "s_mov_b32 s84, 102\n"
      "s_cselect_b32 s84, 51, s84\n"
      "s_add_u32 s84, s84, s83\n"
      "s_lshl_b32 s85, s39, 2\n"
      "s_add_u32 s85, s85, s30\n"
      "s_mul_i32 s85, s85, 161\n"
      "s_add_u32 s84, s84, s85\n"
      "s_lshl_b32 s84, s84, 10\n"
      "s_cmp_lt_u32 s83, 59\n"
      "s_cselect_b32 s5, s84, 0x7ff00000\n"
      "buffer_store_dwordx4 v[64:67], v5, s[20:23], s5 offen sc1\n"
      "s_waitcnt lgkmcnt(0)\n"
      "v_pk_add_f32 v[140:141], v[140:141], v[142:143] op_sel:[0,0] op_sel_hi:[1,1]\n"
      "v_pk_add_f32 v[144:145], v[144:145], v[146:147] op_sel:[0,0] op_sel_hi:[1,1]\n"
      "v_pk_add_f32 v[140:141], v[140:141], v[144:145] op_sel:[0,0] op_sel_hi:[1,1]\n"
      "v_mul_f32 v140, 0x3b000000, v140\n"
      "v_mul_f32 v141, 0x3b000000, v141\n"
      "v_fma_f32 v141, -v140, v140, v141\n"
      "v_max_f32 v141, 0, v141\n"
      "v_add_f32 v141, 0x3727c5ac, v141\n"
      "v_rsq_f32 v142, v141\n"
      "v_sqrt_f32 v143, v141\n"
      "v_lshlrev_b32 v50, 2, v4\n"
      "v_min_u32 v50, 316, v50\n"
      "v_add_u32 v50, 159744, v50\n"
      "v_cmp_gt_u32 vcc, 80, v4\n"
      "s_nop 1\n"
      "s_nop 2\n"
      "s_and_saveexec_b64 s[6:7], vcc\n"
      "ds_write_b32 v50, v140\n"
      "ds_write_b32 v50, v142 offset:320\n"
      "ds_write_b32 v50, v143 offset:640\n"
      "s_mov_b64 exec, -1\n"
      "s_and_b32 s83, s96, 7\n"
      "s_cmp_lg_u32 s83, 0\n"
      "s_cbranch_scc1 Lg1_%=\n"
      "s_waitcnt vmcnt(0) lgkmcnt(0)\n"
      "v_lshrrev_b32 v50, 2, v4\n"
      "v_lshlrev_b32 v50, 1, v50\n"
      "v_add_u32 v50, 161280, v50\n"
      "ds_read_u16 v60, v50 offset:0\n"
      "ds_read_u16 v61, v50 offset:128\n"
      "ds_read_u16 v62, v50 offset:256\n"
      "ds_read_u16 v63, v50 offset:384\n"
      "ds_read_u16 v64, v50 offset:512\n"
      "ds_read_u16 v65, v50 offset:640\n"
      "ds_read_u16 v66, v50 offset:768\n"
      "ds_read_u16 v67, v50 offset:896\n"
      "s_waitcnt lgkmcnt(0)\n"
      "v_lshlrev_b32 v60, 16, v60\n"
      "v_lshlrev_b32 v61, 16, v61\n"
      "v_lshlrev_b32 v62, 16, v62\n"
      "v_lshlrev_b32 v63, 16, v63\n"
      "v_lshlrev_b32 v64, 16, v64\n"
      "v_lshlrev_b32 v65, 16, v65\n"
      "v_lshlrev_b32 v66, 16, v66\n"
      "v_lshlrev_b32 v67, 16, v67\n"
      "v_lshlrev_b32 v148, 16, v68\n"
      "v_and_b32 v149, 0xffff0000, v68\n"
      "v_pk_mul_f32 v[140:141], v[148:149], v[60:61] op_sel:[0,0] op_sel_hi:[1,0]\n"
      "v_lshlrev_b32 v148, 16, v69\n"
      "v_and_b32 v149, 0xffff0000, v69\n"
      "v_pk_mul_f32 v[142:143], v[148:149], v[60:61] op_sel:[0,0] op_sel_hi:[1,0]\n"
      "v_lshlrev_b32 v148, 16, v70\n"
      "v_and_b32 v149, 0xffff0000, v70\n"
      "v_pk_mul_f32 v[144:145], v[148:149], v[60:61] op_sel:[0,0] op_sel_hi:[1,0]\n"
      "v_lshlrev_b32 v148, 16, v71\n"
      "v_and_b32 v149, 0xffff0000, v71\n"
      "v_pk_mul_f32 v[146:147], v[148:149], v[60:61] op_sel:[0,0] op_sel_hi:[1,0]\n"
      "v_lshlrev_b32 v148, 16, v72\n"
      "v_and_b32 v149, 0xffff0000, v72\n"
      "v_pk_fma_f32 v[140:141], v[148:149], v[60:61], v[140:141] op_sel:[0,1,0] op_sel_hi:[1,1,1]\n"
      "v_lshlrev_b32 v148, 16, v73\n"
      "v_and_b32 v149, 0xffff0000, v73\n"
      "v_pk_fma_f32 v[142:143], v[148:149], v[60:61], v[142:143] op_sel:[0,1,0] op_sel_hi:[1,1,1]\n"
      "v_lshlrev_b32 v148, 16, v74\n"
      "v_and_b32 v149, 0xffff0000, v74\n"
      "v_pk_fma_f32 v[144:145], v[148:149], v[60:61], v[144:145] op_sel:[0,1,0] op_sel_hi:[1,1,1]\n"
      "v_lshlrev_b32 v148, 16, v75\n"
      "v_and_b32 v149, 0xffff0000, v75\n"
      "v_pk_fma_f32 v[146:147], v[148:149], v[60:61], v[146:147] op_sel:[0,1,0] op_sel_hi:[1,1,1]\n"
      "v_lshlrev_b32 v148, 16, v76\n"
      "v_and_b32 v149, 0xffff0000, v76\n"
      "v_pk_fma_f32 v[140:141], v[148:149], v[62:63], v[140:141] op_sel:[0,0,0] op_sel_hi:[1,0,1]\n"
      "v_lshlrev_b32 v148, 16, v77\n"
      "v_and_b32 v149, 0xffff0000, v77\n"
      "v_pk_fma_f32 v[142:143], v[148:149], v[62:63], v[142:143] op_sel:[0,0,0] op_sel_hi:[1,0,1]\n"
      "v_lshlrev_b32 v148, 16, v78\n"
      "v_and_b32 v149, 0xffff0000, v78\n"
      "v_pk_fma_f32 v[144:145], v[148:149], v[62:63], v[144:145] op_sel:[0,0,0] op_sel_hi:[1,0,1]\n"
      "v_lshlrev_b32 v148, 16, v79\n"
      "v_and_b32 v149, 0xffff0000, v79\n"
      "v_pk_fma_f32 v[146:147], v[148:149], v[62:63], v[146:147] op_sel:[0,0,0] op_sel_hi:[1,0,1]\n"
      "v_lshlrev_b32 v148, 16, v80\n"
      "v_and_b32 v149, 0xffff0000, v80\n"
      "v_pk_fma_f32 v[140:141], v[148:149], v[62:63], v[140:141] op_sel:[0,1,0] op_sel_hi:[1,1,1]\n"
      "v_lshlrev_b32 v148, 16, v81\n"
      "v_and_b32 v149, 0xffff0000, v81\n"
      "v_pk_fma_f32 v[142:143], v[148:149], v[62:63], v[142:143] op_sel:[0,1,0] op_sel_hi:[1,1,1]\n"
      "v_lshlrev_b32 v148, 16, v82\n"
      "v_and_b32 v149, 0xffff0000, v82\n"
      "v_pk_fma_f32 v[144:145], v[148:149], v[62:63], v[144:145] op_sel:[0,1,0] op_sel_hi:[1,1,1]\n"
      "v_lshlrev_b32 v148, 16, v83\n"
      "v_and_b32 v149, 0xffff0000, v83\n"
      "v_pk_fma_f32 v[146:147], v[148:149], v[62:63], v[146:147] op_sel:[0,1,0] op_sel_hi:[1,1,1]\n"
      "v_lshlrev_b32 v148, 16, v84\n"
      "v_and_b32 v149, 0xffff0000, v84\n"
      "v_pk_fma_f32 v[140:141], v[148:149], v[64:65], v[140:141] op_sel:[0,0,0] op_sel_hi:[1,0,1]\n"
      "v_lshlrev_b32 v148, 16, v85\n"
      "v_and_b32 v149, 0xffff0000, v85\n"
      "v_pk_fma_f32 v[142:143], v[148:149], v[64:65], v[142:143] op_sel:[0,0,0] op_sel_hi:[1,0,1]\n"
      "v_lshlrev_b32 v148, 16, v86\n"
      "v_and_b32 v149, 0xffff0000, v86\n"
      "v_pk_fma_f32 v[144:145], v[148:149], v[64:65], v[144:145] op_sel:[0,0,0] op_sel_hi:[1,0,1]\n"
      "v_lshlrev_b32 v148, 16, v87\n"
      "v_and_b32 v149, 0xffff0000, v87\n"
      "v_pk_fma_f32 v[146:147], v[148:149], v[64:65], v[146:147] op_sel:[0,0,0] op_sel_hi:[1,0,1]\n"
      "v_lshlrev_b32 v148, 16, v88\n"
      "v_and_b32 v149, 0xffff0000, v88\n"
      "v_pk_fma_f32 v[140:141], v[148:149], v[64:65], v[140:141] op_sel:[0,1,0] op_sel_hi:[1,1,1]\n"
      "v_lshlrev_b32 v148, 16, v89\n"
      "v_and_b32 v149, 0xffff0000, v89\n"
      "v_pk_fma_f32 v[142:143], v[148:149], v[64:65], v[142:143] op_sel:[0,1,0] op_sel_hi:[1,1,1]\n"
      "v_lshlrev_b32 v148, 16, v90\n"
      "v_and_b32 v149, 0xffff0000, v90\n"
      "v_pk_fma_f32 v[144:145], v[148:149], v[64:65], v[144:145] op_sel:[0,1,0] op_sel_hi:[1,1,1]\n"
      "v_lshlrev_b32 v148, 16, v91\n"
      "v_and_b32 v149, 0xffff0000, v91\n"
      "v_pk_fma_f32 v[146:147], v[148:149], v[64:65], v[146:147] op_sel:[0,1,0] op_sel_hi:[1,1,1]\n"
      "v_lshlrev_b32 v148, 16, v92\n"
      "v_and_b32 v149, 0xffff0000, v92\n"
      "v_pk_fma_f32 v[140:141], v[148:149], v[66:67], v[140:141] op_sel:[0,0,0] op_sel_hi:[1,0,1]\n"
      "v_lshlrev_b32 v148, 16, v93\n"
      "v_and_b32 v149, 0xffff0000, v93\n"
      "v_pk_fma_f32 v[142:143], v[148:149], v[66:67], v[142:143] op_sel:[0,0,0] op_sel_hi:[1,0,1]\n"
      "v_lshlrev_b32 v148, 16, v94\n"
      "v_and_b32 v149, 0xffff0000, v94\n"
      "v_pk_fma_f32 v[144:145], v[148:149], v[66:67], v[144:145] op_sel:[0,0,0] op_sel_hi:[1,0,1]\n"
      "v_lshlrev_b32 v148, 16, v95\n"
      "v_and_b32 v149, 0xffff0000, v95\n"
      "v_pk_fma_f32 v[146:147], v[148:149], v[66:67], v[146:147] op_sel:[0,0,0] op_sel_hi:[1,0,1]\n"
      "v_lshlrev_b32 v148, 16, v96\n"
      "v_and_b32 v149, 0xffff0000, v96\n"
      "v_pk_fma_f32 v[140:141], v[148:149], v[66:67], v[140:141] op_sel:[0,1,0] op_sel_hi:[1,1,1]\n"
      "v_lshlrev_b32 v148, 16, v97\n"
      "v_and_b32 v149, 0xffff0000, v97\n"
      "v_pk_fma_f32 v[142:143], v[148:149], v[66:67], v[142:143] op_sel:[0,1,0] op_sel_hi:[1,1,1]\n"
      "v_lshlrev_b32 v148, 16, v98\n"
      "v_and_b32 v149, 0xffff0000, v98\n"
      "v_pk_fma_f32 v[144:145], v[148:149], v[66:67], v[144:145] op_sel:[0,1,0] op_sel_hi:[1,1,1]\n"
      "v_lshlrev_b32 v148, 16, v99\n"
      "v_and_b32 v149, 0xffff0000, v99\n"
      "v_pk_fma_f32 v[146:147], v[148:149], v[66:67], v[146:147] op_sel:[0,1,0] op_sel_hi:[1,1,1]\n"
      "v_add_f32_dpp v140, v140, v140 row_shr:4 row_mask:0xf bank_mask:0xf bound_ctrl:1\n"
      "s_nop 1\n"
      "v_add_f32_dpp v140, v140, v140 row_shr:8 row_mask:0xf bank_mask:0xf bound_ctrl:1\n"
      "v_add_f32_dpp v141, v141, v141 row_shr:4 row_mask:0xf bank_mask:0xf bound_ctrl:1\n"
      "s_nop 1\n"
      "v_add_f32_dpp v141, v141, v141 row_shr:8 row_mask:0xf bank_mask:0xf bound_ctrl:1\n"
      "v_add_f32_dpp v142, v142, v142 row_shr:4 row_mask:0xf bank_mask:0xf bound_ctrl:1\n"
      "s_nop 1\n"
      "v_add_f32_dpp v142, v142, v142 row_shr:8 row_mask:0xf bank_mask:0xf bound_ctrl:1\n"
      "v_add_f32_dpp v143, v143, v143 row_shr:4 row_mask:0xf bank_mask:0xf bound_ctrl:1\n"
      "s_nop 1\n"
      "v_add_f32_dpp v143, v143, v143 row_shr:8 row_mask:0xf bank_mask:0xf bound_ctrl:1\n"
      "v_add_f32_dpp v144, v144, v144 row_shr:4 row_mask:0xf bank_mask:0xf bound_ctrl:1\n"
      "s_nop 1\n"
      "v_add_f32_dpp v144, v144, v144 row_shr:8 row_mask:0xf bank_mask:0xf bound_ctrl:1\n"
      "v_add_f32_dpp v145, v145, v145 row_shr:4 row_mask:0xf bank_mask:0xf bound_ctrl:1\n"
      "s_nop 1\n"
      "v_add_f32_dpp v145, v145, v145 row_shr:8 row_mask:0xf bank_mask:0xf bound_ctrl:1\n"
      "v_add_f32_dpp v146, v146, v146 row_shr:4 row_mask:0xf bank_mask:0xf bound_ctrl:1\n"
      "s_nop 1\n"
      "v_add_f32_dpp v146, v146, v146 row_shr:8 row_mask:0xf bank_mask:0xf bound_ctrl:1\n"
      "v_add_f32_dpp v147, v147, v147 row_shr:4 row_mask:0xf bank_mask:0xf bound_ctrl:1\n"
      "s_nop 1\n"
      "v_add_f32_dpp v147, v147, v147 row_shr:8 row_mask:0xf bank_mask:0xf bound_ctrl:1\n"
      "v_mov_b32 v148, v140\n"
      "s_nop 1\n"
      "v_permlane16_swap_b32 v140, v148\n"
      "v_add_f32 v140, v140, v148\n"
      "v_mov_b32 v148, v140\n"
      "s_nop 1\n"
      "v_permlane32_swap_b32 v140, v148\n"
      "v_add_f32 v140, v140, v148\n"
      "v_mov_b32 v148, v141\n"
      "s_nop 1\n"
      "v_permlane16_swap_b32 v141, v148\n"
      "v_add_f32 v141, v141, v148\n"
      "v_mov_b32 v148, v141\n"
      "s_nop 1\n"
      "v_permlane32_swap_b32 v141, v148\n"
      "v_add_f32 v141, v141, v148\n"
      "v_mov_b32 v148, v142\n"
      "s_nop 1\n"
      "v_permlane16_swap_b32 v142, v148\n"
      "v_add_f32 v142, v142, v148\n"
      "v_mov_b32 v148, v142\n"
      "s_nop 1\n"
      "v_permlane32_swap_b32 v142, v148\n"
      "v_add_f32 v142, v142, v148\n"
      "v_mov_b32 v148, v143\n"
      "s_nop 1\n"
      "v_permlane16_swap_b32 v143, v148\n"
      "v_add_f32 v143, v143, v148\n"
      "v_mov_b32 v148, v143\n"
      "s_nop 1\n"
      "v_permlane32_swap_b32 v143, v148\n"
      "v_add_f32 v143, v143, v148\n"
      "v_mov_b32 v148, v144\n"
      "s_nop 1\n"
      "v_permlane16_swap_b32 v144, v148\n"
      "v_add_f32 v144, v144, v148\n"
      "v_mov_b32 v148, v144\n"
      "s_nop 1\n"
      "v_permlane32_swap_b32 v144, v148\n"
      "v_add_f32 v144, v144, v148\n"
      "v_mov_b32 v148, v145\n"
      "s_nop 1\n"
      "v_permlane16_swap_b32 v145, v148\n"
      "v_add_f32 v145, v145, v148\n"
      "v_mov_b32 v148, v145\n"
      "s_nop 1\n"
      "v_permlane32_swap_b32 v145, v148\n"
      "v_add_f32 v145, v145, v148\n"
      "v_mov_b32 v148, v146\n"
      "s_nop 1\n"
      "v_permlane16_swap_b32 v146, v148\n"
      "v_add_f32 v146, v146, v148\n"
      "v_mov_b32 v148, v146\n"
      "s_nop 1\n"
      "v_permlane32_swap_b32 v146, v148\n"
      "v_add_f32 v146, v146, v148\n"
      "v_mov_b32 v148, v147\n"
      "s_nop 1\n"
      "v_permlane16_swap_b32 v147, v148\n"
      "v_add_f32 v147, v147, v148\n"
      "v_mov_b32 v148, v147\n"
      "s_nop 1\n"
      "v_permlane32_swap_b32 v147, v148\n"
      "v_add_f32 v147, v147, v148\n"
      "v_and_b32 v50, 3, v7\n"
      "v_lshlrev_b32 v50, 5, v50\n"
      "s_lshl_b32 s83, s30, 7\n"
      "s_add_u32 s83, s83, 98304\n"
      "v_add_u32 v50, s83, v50\n"
      "s_mov_b64 exec, 0xf000\n"
      "ds_write_b128 v50, v[140:143]\n"
      "ds_write_b128 v50, v[144:147] offset:16\n"
      "s_mov_b64 exec, -1\n"
      "s_waitcnt lgkmcnt(0)\n"
      "s_barrier\n"
      "v_and_b32 v50, 31, v7\n"
      "v_lshlrev_b32 v50, 2, v50\n"
      "v_add_u32 v51, 98304, v50\n"
      "ds_read_b32 v140, v51 offset:0\n"
      "ds_read_b32 v141, v51 offset:128\n"
      "ds_read_b32 v142, v51 offset:256\n"
      "ds_read_b32 v143, v51 offset:384\n"
      "s_waitcnt lgkmcnt(0)\n"
      "v_add_f32 v140, v140, v141\n"
      "v_add_f32 v142, v142, v143\n"
      "v_add_f32 v140, v140, v142\n"
      "v_cvt_pk_bf16_f32 v140, v140, v140\n"
      "v_lshlrev_b32 v140, 16, v140\n"
      "s_lshr_b32 s83, s96, 3\n"
      "s_lshr_b32 s84, s83, 1\n"
      "s_lshl_b32 s84, s84, 11\n"
      "s_and_b32 s83, s83, 1\n"
      "s_lshl_b32 s83, s83, 7\n"
      "s_add_u32 s83, s83, s84\n"
      "s_lshl_b32 s84, s39, 8\n"
      "s_add_u32 s83, s83, s84\n"
      "s_add_u32 s83, s83, 5275648\n"
      "s_cmp_eq_u32 s30, 0\n"
      "s_cselect_b64 exec, 0xffffffff, 0\n"
      "s_cbranch_execz Lg2_%=\n"
      "buffer_store_dword v140, v50, s[20:23], s83 offen sc1\n"
      "Lg2_%=:\n"
      "s_mov_b64 exec, -1\n"
      "s_waitcnt vmcnt(0)\n"
      "s_nop 0\n"
      "Lg1_%=:\n"
      "s_waitcnt vmcnt(0)\n"
      "s_waitcnt lgkmcnt(0)\n"
      "s_barrier\n"
      "s_cmp_lg_u32 s30, 0\n"
      "s_cbranch_scc1 Lfb_%=\n"
      "s_lshl_b32 s83, s39, 7\n"
      "s_lshl_b32 s84, s96, 2\n"
      "s_add_u32 s83, s83, s84\n"
      "s_add_u32 s83, s83, 5313536\n"
      "v_mov_b32 v60, 0\n"
      "v_mov_b32 v61, s31\n"
      "s_mov_b64 exec, 1\n"
      "buffer_store_dword v61, v60, s[20:23], s83 offen sc1\n"
      "s_mov_b64 exec, -1\n"
      "v_and_b32 v60, 15, v7\n"
      "v_lshlrev_b32 v60, 2, v60\n"
      "s_lshl_b32 s83, s29, 7\n"
      "s_add_u32 s83, s83, 5312512\n"
      "s_mov_b32 s86, 0\n"
      "s_nop 0\n"
      "Lpa_%=:\n"
      "buffer_load_dword v61, v60, s[20:23], s83 offen sc1\n"
      "s_add_u32 s86, s86, 1\n"
      "s_waitcnt vmcnt(0)\n"
      "v_cmp_ne_u32 vcc, s31, v61\n"
      "s_cmp_gt_u32 s86, 0x10000\n"
      "s_cbranch_scc1 Lfb_%=\n"
      "s_nop 3\n"
      "s_cbranch_vccnz Lpa_%=\n"
      "s_nop 0\n"
      "Lfb_%=:\n"
      "s_barrier\n"
      "s_mov_b32 s24, s78\n"
      "s_and_b32 s25, s79, 0xffff\n"
      "s_mov_b32 s26, 0x200000\n"
      "s_mov_b32 s27, 0x20000\n"
      "s_lshl_b32 s83, s29, 2\n"
      "s_add_u32 s83, s83, s30\n"
      "s_mul_i32 s82, s83, 164864\n"
      "buffer_load_dwordx4 a[0:3], v5, s[20:23], s82 offen\n"
      "buffer_load_dwordx4 a[4:7], v5, s[20:23], s82 offen offset:1024\n"
      "buffer_load_dwordx4 a[8:11], v5, s[20:23], s82 offen offset:2048\n"
      "buffer_load_dwordx4 a[12:15], v5, s[20:23], s82 offen offset:3072\n"
      "s_add_u32 s82, s82, 4096\n"
      "buffer_load_dwordx4 a[16:19], v5, s[20:23], s82 offen\n"
      "buffer_load_dwordx4 a[20:23], v5, s[20:23], s82 offen offset:1024\n"
      "buffer_load_dwordx4 a[24:27], v5, s[20:23], s82 offen offset:2048\n"
      "buffer_load_dwordx4 a[28:31], v5, s[20:23], s82 offen offset:3072\n"
      "s_add_u32 s82, s82, 4096\n"
      "buffer_load_dwordx4 a[32:35], v5, s[20:23], s82 offen\n"
      "buffer_load_dwordx4 a[36:39], v5, s[20:23], s82 offen offset:1024\n"
      "buffer_load_dwordx4 a[40:43], v5, s[20:23], s82 offen offset:2048\n"
      "buffer_load_dwordx4 a[44:47], v5, s[20:23], s82 offen offset:3072\n"
      "s_add_u32 s82, s82, 4096\n"
      "buffer_load_dwordx4 a[48:51], v5, s[20:23], s82 offen\n"
      "buffer_load_dwordx4 a[52:55], v5, s[20:23], s82 offen offset:1024\n"
      "buffer_load_dwordx4 a[56:59], v5, s[20:23], s82 offen offset:2048\n"
      "; P1\n"
      "v_lshlrev_b32 v56, 2, v48\n"
      "v_add_u32 v56, 159744, v56\n"
      "v_lshlrev_b32 v105, 4, v49\n"
      "v_add_u32 v105, 159744, v105\n"
      "ds_read_b32 v106, v56\n"
      "ds_read_b32 v151, v56 offset:640\n"
      "ds_read_b32 v156, v56 offset:320\n"
      "ds_read_b32 v107, v56 offset:64\n"
      "ds_read_b32 v152, v56 offset:704\n"
      "ds_read_b32 v157, v56 offset:384\n"
      "ds_read_b32 v148, v56 offset:128\n"
      "ds_read_b32 v153, v56 offset:768\n"
      "ds_read_b32 v158, v56 offset:448\n"
      "ds_read_b32 v149, v56 offset:192\n"
      "ds_read_b32 v154, v56 offset:832\n"
      "ds_read_b32 v159, v56 offset:512\n"
      "ds_read_b32 v150, v56 offset:256\n"
      "ds_read_b32 v155, v56 offset:896\n"
      "ds_read_b32 v160, v56 offset:576\n"
      "s_waitcnt vmcnt(14)\n"
      "s_waitcnt vmcnt(13)\n"
      "s_waitcnt vmcnt(12)\n"
      "v_mfma_f32_16x16x32_bf16 v[164:167], a[0:3], v[128:131], 0\n"
      "buffer_load_dwordx4 a[60:63], v5, s[20:23], s82 offen offset:3072\n"
      "v_mfma_f32_16x16x32_bf16 v[168:171], a[0:3], v[132:135], 0\n"
      "v_mfma_f32_16x16x32_bf16 v[172:175], a[4:7], v[128:131], 0\n"
      "v_mfma_f32_16x16x32_bf16 v[176:179], a[4:7], v[132:135], 0\n"
      "v_mfma_f32_16x16x32_bf16 v[180:183], v[128:131], a[8:11], 0\n"
      "v_mfma_f32_16x16x32_bf16 v[184:187], v[132:135], a[8:11], 0\n"
      "s_waitcnt lgkmcnt(14)\n"
      "ds_read_b128 v[188:191], v105\n"
      "s_waitcnt lgkmcnt(14)\n"
      "ds_read_b128 v[196:199], v105 offset:640\n"
      "s_waitcnt lgkmcnt(2)\n"
      "ds_read_b128 v[192:195], v105 offset:64\n"
      "ds_read_b128 v[200:203], v105 offset:704\n"
      "v_pk_mul_f32 v[72:73], v[150:151], v[172:173] op_sel:[1,0] op_sel_hi:[1,1]\n"
      "s_add_u32 s82, s82, 4096\n"
      "buffer_load_dwordx4 a[64:67], v5, s[20:23], s82 offen\n"
      "v_pk_mul_f32 v[74:75], v[150:151], v[174:175] op_sel:[1,0] op_sel_hi:[1,1]\n"
      "v_pk_fma_f32 v[72:73], v[106:107], v[176:177], v[72:73] op_sel:[0,0,0] op_sel_hi:[0,1,1] neg_lo:[1,0,0] neg_hi:[1,0,0]\n"
      "v_pk_fma_f32 v[74:75], v[106:107], v[178:179], v[74:75] op_sel:[0,0,0] op_sel_hi:[0,1,1] neg_lo:[1,0,0] neg_hi:[1,0,0]\n"
      "s_waitcnt lgkmcnt(2)\n"
      "v_pk_mul_f32 v[92:93], v[196:197], v[180:181] op_sel:[0,0] op_sel_hi:[1,0]\n"
      "v_pk_mul_f32 v[94:95], v[198:199], v[180:181] op_sel:[0,0] op_sel_hi:[1,0]\n"
      "v_pk_fma_f32 v[92:93], v[188:189], v[184:185], v[92:93] op_sel:[0,0,0] op_sel_hi:[1,0,1] neg_lo:[1,0,0] neg_hi:[1,0,0]\n"
      "v_pk_fma_f32 v[94:95], v[190:191], v[184:185], v[94:95] op_sel:[0,0,0] op_sel_hi:[1,0,1] neg_lo:[1,0,0] neg_hi:[1,0,0]\n"
      "ds_read_b128 v[188:191], v105 offset:128\n"
      "ds_read_b128 v[196:199], v105 offset:768\n"
      "v_pk_mul_f32 v[52:53], v[152:153], v[164:165] op_sel:[0,0] op_sel_hi:[0,1]\n"
      "v_pk_mul_f32 v[54:55], v[152:153], v[166:167] op_sel:[0,0] op_sel_hi:[0,1]\n"
      "v_pk_mul_f32 v[76:77], v[152:153], v[172:173] op_sel:[0,0] op_sel_hi:[0,1]\n"
      "v_pk_mul_f32 v[78:79], v[152:153], v[174:175] op_sel:[0,0] op_sel_hi:[0,1]\n"
      "v_pk_fma_f32 v[52:53], v[106:107], v[168:169], v[52:53] op_sel:[1,0,0] op_sel_hi:[1,1,1] neg_lo:[1,0,0] neg_hi:[1,0,0]\n"
      "v_pk_fma_f32 v[54:55], v[106:107], v[170:171], v[54:55] op_sel:[1,0,0] op_sel_hi:[1,1,1] neg_lo:[1,0,0] neg_hi:[1,0,0]\n"
      "v_pk_fma_f32 v[76:77], v[106:107], v[176:177], v[76:77] op_sel:[1,0,0] op_sel_hi:[1,1,1] neg_lo:[1,0,0] neg_hi:[1,0,0]\n"
      "v_pk_fma_f32 v[78:79], v[106:107], v[178:179], v[78:79] op_sel:[1,0,0] op_sel_hi:[1,1,1] neg_lo:[1,0,0] neg_hi:[1,0,0]\n"
      "s_waitcnt lgkmcnt(2)\n"
      "v_pk_mul_f32 v[96:97], v[200:201], v[180:181] op_sel:[0,0] op_sel_hi:[1,0]\n"
      "buffer_load_dwordx4 a[68:71], v5, s[20:23], s82 offen offset:1024\n"
      "v_pk_mul_f32 v[98:99], v[202:203], v[180:181] op_sel:[0,0] op_sel_hi:[1,0]\n"
      "v_pk_fma_f32 v[96:97], v[192:193], v[184:185], v[96:97] op_sel:[0,0,0] op_sel_hi:[1,0,1] neg_lo:[1,0,0] neg_hi:[1,0,0]\n"
      "v_pk_fma_f32 v[98:99], v[194:195], v[184:185], v[98:99] op_sel:[0,0,0] op_sel_hi:[1,0,1] neg_lo:[1,0,0] neg_hi:[1,0,0]\n"
      "ds_read_b128 v[192:195], v105 offset:192\n"
      "ds_read_b128 v[200:203], v105 offset:832\n"
      "v_pk_mul_f32 v[60:61], v[152:153], v[164:165] op_sel:[1,0] op_sel_hi:[1,1]\n"
      "v_pk_mul_f32 v[62:63], v[152:153], v[166:167] op_sel:[1,0] op_sel_hi:[1,1]\n"
      "v_pk_mul_f32 v[80:81], v[152:153], v[172:173] op_sel:[1,0] op_sel_hi:[1,1]\n"
      "v_pk_mul_f32 v[82:83], v[152:153], v[174:175] op_sel:[1,0] op_sel_hi:[1,1]\n"
      "v_pk_fma_f32 v[60:61], v[148:149], v[168:169], v[60:61] op_sel:[0,0,0] op_sel_hi:[0,1,1] neg_lo:[1,0,0] neg_hi:[1,0,0]\n"
      "v_pk_fma_f32 v[62:63], v[148:149], v[170:171], v[62:63] op_sel:[0,0,0] op_sel_hi:[0,1,1] neg_lo:[1,0,0] neg_hi:[1,0,0]\n"
      "v_pk_fma_f32 v[80:81], v[148:149], v[176:177], v[80:81] op_sel:[0,0,0] op_sel_hi:[0,1,1] neg_lo:[1,0,0] neg_hi:[1,0,0]\n"
      "v_pk_fma_f32 v[82:83], v[148:149], v[178:179], v[82:83] op_sel:[0,0,0] op_sel_hi:[0,1,1] neg_lo:[1,0,0] neg_hi:[1,0,0]\n"
      "s_waitcnt lgkmcnt(2)\n"
      "v_pk_mul_f32 v[136:137], v[196:197], v[180:181] op_sel:[0,0] op_sel_hi:[1,0]\n"
      "v_pk_mul_f32 v[138:139], v[198:199], v[180:181] op_sel:[0,0] op_sel_hi:[1,0]\n"
      "v_pk_fma_f32 v[136:137], v[188:189], v[184:185], v[136:137] op_sel:[0,0,0] op_sel_hi:[1,0,1] neg_lo:[1,0,0] neg_hi:[1,0,0]\n"
      "v_pk_fma_f32 v[138:139], v[190:191], v[184:185], v[138:139] op_sel:[0,0,0] op_sel_hi:[1,0,1] neg_lo:[1,0,0] neg_hi:[1,0,0]\n"
      "ds_read_b128 v[188:191], v105 offset:256\n"
      "ds_read_b128 v[196:199], v105 offset:896\n"
      "v_pk_mul_f32 v[64:65], v[154:155], v[164:165] op_sel:[0,0] op_sel_hi:[0,1]\n"
      "buffer_load_dwordx4 a[72:75], v5, s[20:23], s82 offen offset:2048\n"
      "v_pk_mul_f32 v[66:67], v[154:155], v[166:167] op_sel:[0,0] op_sel_hi:[0,1]\n"
      "v_pk_mul_f32 v[84:85], v[154:155], v[172:173] op_sel:[0,0] op_sel_hi:[0,1]\n"
      "v_pk_mul_f32 v[86:87], v[154:155], v[174:175] op_sel:[0,0] op_sel_hi:[0,1]\n"
      "v_pk_fma_f32 v[64:65], v[148:149], v[168:169], v[64:65] op_sel:[1,0,0] op_sel_hi:[1,1,1] neg_lo:[1,0,0] neg_hi:[1,0,0]\n"
      "v_pk_fma_f32 v[66:67], v[148:149], v[170:171], v[66:67] op_sel:[1,0,0] op_sel_hi:[1,1,1] neg_lo:[1,0,0] neg_hi:[1,0,0]\n"
      "v_pk_fma_f32 v[84:85], v[148:149], v[176:177], v[84:85] op_sel:[1,0,0] op_sel_hi:[1,1,1] neg_lo:[1,0,0] neg_hi:[1,0,0]\n"
      "v_pk_fma_f32 v[86:87], v[148:149], v[178:179], v[86:87] op_sel:[1,0,0] op_sel_hi:[1,1,1] neg_lo:[1,0,0] neg_hi:[1,0,0]\n"
      "s_waitcnt lgkmcnt(2)\n"
      "v_pk_mul_f32 v[140:141], v[200:201], v[180:181] op_sel:[0,0] op_sel_hi:[1,0]\n"
      "v_pk_mul_f32 v[142:143], v[202:203], v[180:181] op_sel:[0,0] op_sel_hi:[1,0]\n"
      "v_pk_fma_f32 v[140:141], v[192:193], v[184:185], v[140:141] op_sel:[0,0,0] op_sel_hi:[1,0,1] neg_lo:[1,0,0] neg_hi:[1,0,0]\n"
      "v_pk_fma_f32 v[142:143], v[194:195], v[184:185], v[142:143] op_sel:[0,0,0] op_sel_hi:[1,0,1] neg_lo:[1,0,0] neg_hi:[1,0,0]\n"
      "v_pk_mul_f32 v[68:69], v[154:155], v[164:165] op_sel:[1,0] op_sel_hi:[1,1]\n"
      "v_pk_mul_f32 v[70:71], v[154:155], v[166:167] op_sel:[1,0] op_sel_hi:[1,1]\n"
      "v_pk_mul_f32 v[88:89], v[154:155], v[172:173] op_sel:[1,0] op_sel_hi:[1,1]\n"
      "v_pk_mul_f32 v[90:91], v[154:155], v[174:175] op_sel:[1,0] op_sel_hi:[1,1]\n"
      "v_pk_fma_f32 v[68:69], v[150:151], v[168:169], v[68:69] op_sel:[0,0,0] op_sel_hi:[0,1,1] neg_lo:[1,0,0] neg_hi:[1,0,0]\n"
      "v_pk_fma_f32 v[70:71], v[150:151], v[170:171], v[70:71] op_sel:[0,0,0] op_sel_hi:[0,1,1] neg_lo:[1,0,0] neg_hi:[1,0,0]\n"
      "v_pk_fma_f32 v[88:89], v[150:151], v[176:177], v[88:89] op_sel:[0,0,0] op_sel_hi:[0,1,1] neg_lo:[1,0,0] neg_hi:[1,0,0]\n"
      "buffer_load_dwordx4 a[76:79], v5, s[20:23], s82 offen offset:3072\n"
      "v_pk_fma_f32 v[90:91], v[150:151], v[178:179], v[90:91] op_sel:[0,0,0] op_sel_hi:[0,1,1] neg_lo:[1,0,0] neg_hi:[1,0,0]\n"
      "s_waitcnt lgkmcnt(0)\n"
      "v_pk_mul_f32 v[144:145], v[196:197], v[180:181] op_sel:[0,0] op_sel_hi:[1,0]\n"
      "v_pk_mul_f32 v[146:147], v[198:199], v[180:181] op_sel:[0,0] op_sel_hi:[1,0]\n"
      "v_pk_fma_f32 v[144:145], v[188:189], v[184:185], v[144:145] op_sel:[0,0,0] op_sel_hi:[1,0,1] neg_lo:[1,0,0] neg_hi:[1,0,0]\n"
      "v_pk_fma_f32 v[146:147], v[190:191], v[184:185], v[146:147] op_sel:[0,0,0] op_sel_hi:[1,0,1] neg_lo:[1,0,0] neg_hi:[1,0,0]\n"
      "ds_read_b128 v[204:207], v57\n"
      "ds_read_b128 v[208:211], v57 offset:16384\n"
      "ds_read_b128 v[212:215], v57 offset:32768\n"
      "ds_read_b128 v[216:219], v57 offset:49152\n"
      "ds_read_b128 v[220:223], v101\n"
      "s_waitcnt vmcnt(16)\n"
      "s_waitcnt vmcnt(15)\n"
      "s_waitcnt vmcnt(14)\n"
      "s_waitcnt lgkmcnt(4)\n"
      "v_mfma_f32_16x16x32_bf16 v[72:75], a[16:19], v[204:207], v[72:75]\n"
      "ds_read_b128 v[224:227], v58\n"
      "v_mfma_f32_16x16x32_bf16 v[92:95], v[204:207], a[20:23], v[92:95]\n"
      "s_add_u32 s82, s82, 4096\n"
      "s_waitcnt lgkmcnt(4)\n"
      "v_mfma_f32_16x16x32_bf16 v[52:55], a[12:15], v[208:211], v[52:55]\n"
      "v_mfma_f32_16x16x32_bf16 v[76:79], a[16:19], v[208:211], v[76:79]\n"
      "ds_read_b128 v[228:231], v58 offset:16384\n"
      "v_mfma_f32_16x16x32_bf16 v[96:99], v[208:211], a[20:23], v[96:99]\n"
      "buffer_load_dwordx4 a[80:83], v5, s[20:23], s82 offen\n"
      "s_waitcnt lgkmcnt(4)\n"
      "v_mfma_f32_16x16x32_bf16 v[60:63], a[12:15], v[212:215], v[60:63]\n"
      "v_mfma_f32_16x16x32_bf16 v[80:83], a[16:19], v[212:215], v[80:83]\n"
      "ds_read_b128 v[232:235], v58 offset:32768\n"
      "v_mfma_f32_16x16x32_bf16 v[136:139], v[212:215], a[20:23], v[136:139]\n"
      "buffer_load_dwordx4 a[84:87], v5, s[20:23], s82 offen offset:1024\n"
      "s_waitcnt lgkmcnt(4)\n"
      "v_mfma_f32_16x16x32_bf16 v[64:67], a[12:15], v[216:219], v[64:67]\n"
      "v_mfma_f32_16x16x32_bf16 v[84:87], a[16:19], v[216:219], v[84:87]\n"
      "ds_read_b128 v[236:239], v58 offset:49152\n"
      "v_mfma_f32_16x16x32_bf16 v[140:143], v[216:219], a[20:23], v[140:143]\n"
      "buffer_load_dwordx4 a[88:91], v5, s[20:23], s82 offen offset:2048\n"
      "s_waitcnt lgkmcnt(4)\n"
      "v_mfma_f32_16x16x32_bf16 v[68:71], a[12:15], v[220:223], v[68:71]\n"
      "v_mfma_f32_16x16x32_bf16 v[88:91], a[16:19], v[220:223], v[88:91]\n"
      "ds_read_b128 v[240:243], v102\n"
      "v_mfma_f32_16x16x32_bf16 v[144:147], v[220:223], a[20:23], v[144:147]\n"
      "s_waitcnt vmcnt(16)\n"
      "s_waitcnt vmcnt(15)\n"
      "s_waitcnt vmcnt(14)\n"
      "s_waitcnt lgkmcnt(4)\n"
      "v_mfma_f32_16x16x32_bf16 v[72:75], a[28:31], v[224:227], v[72:75]\n"
      "ds_read_b128 v[204:207], v59\n"
      "v_mfma_f32_16x16x32_bf16 v[92:95], v[224:227], a[32:35], v[92:95]\n"
      "buffer_load_dwordx4 a[92:95], v5, s[20:23], s82 offen offset:3072\n"
      "s_waitcnt lgkmcnt(4)\n"
      "v_mfma_f32_16x16x32_bf16 v[52:55], a[24:27], v[228:231], v[52:55]\n"
      "v_mfma_f32_16x16x32_bf16 v[76:79], a[28:31], v[228:231], v[76:79]\n"
      "ds_read_b128 v[208:211], v59 offset:16384\n"
      "v_mfma_f32_16x16x32_bf16 v[96:99], v[228:231], a[32:35], v[96:99]\n"
      "s_add_u32 s82, s82, 4096\n"
      "s_waitcnt lgkmcnt(4)\n"
      "v_mfma_f32_16x16x32_bf16 v[60:63], a[24:27], v[232:235], v[60:63]\n"
      "v_mfma_f32_16x16x32_bf16 v[80:83], a[28:31], v[232:235], v[80:83]\n"
      "ds_read_b128 v[212:215], v59 offset:32768\n"
      "v_mfma_f32_16x16x32_bf16 v[136:139], v[232:235], a[32:35], v[136:139]\n"
      "buffer_load_dwordx4 a[96:99], v5, s[20:23], s82 offen\n"
      "s_waitcnt lgkmcnt(4)\n"
      "v_mfma_f32_16x16x32_bf16 v[64:67], a[24:27], v[236:239], v[64:67]\n"
      "v_mfma_f32_16x16x32_bf16 v[84:87], a[28:31], v[236:239], v[84:87]\n"
      "ds_read_b128 v[216:219], v59 offset:49152\n"
      "v_mfma_f32_16x16x32_bf16 v[140:143], v[236:239], a[32:35], v[140:143]\n"
      "buffer_load_dwordx4 a[100:103], v5, s[20:23], s82 offen offset:1024\n"
      "s_waitcnt lgkmcnt(4)\n"
      "v_mfma_f32_16x16x32_bf16 v[68:71], a[24:27], v[240:243], v[68:71]\n"
      "v_mfma_f32_16x16x32_bf16 v[88:91], a[28:31], v[240:243], v[88:91]\n"
      "ds_read_b128 v[220:223], v103\n"
      "v_mfma_f32_16x16x32_bf16 v[144:147], v[240:243], a[32:35], v[144:147]\n"
      "s_waitcnt vmcnt(16)\n"
      "s_waitcnt vmcnt(15)\n"
      "s_waitcnt vmcnt(14)\n"
      "s_waitcnt lgkmcnt(4)\n"
      "v_mfma_f32_16x16x32_bf16 v[72:75], a[40:43], v[204:207], v[72:75]\n"
      "ds_read_b128 v[224:227], v100\n"
      "v_mfma_f32_16x16x32_bf16 v[92:95], v[204:207], a[44:47], v[92:95]\n"
      "buffer_load_dwordx4 a[104:107], v5, s[20:23], s82 offen offset:2048\n"
      "s_waitcnt lgkmcnt(4)\n"
      "v_mfma_f32_16x16x32_bf16 v[52:55], a[36:39], v[208:211], v[52:55]\n"
      "v_mfma_f32_16x16x32_bf16 v[76:79], a[40:43], v[208:211], v[76:79]\n"
      "ds_read_b128 v[228:231], v100 offset:16384\n"
      "v_mfma_f32_16x16x32_bf16 v[96:99], v[208:211], a[44:47], v[96:99]\n"
      "buffer_load_dwordx4 a[108:111], v5, s[20:23], s82 offen offset:3072\n"
      "s_waitcnt lgkmcnt(4)\n"
      "v_mfma_f32_16x16x32_bf16 v[60:63], a[36:39], v[212:215], v[60:63]\n"
      "v_mfma_f32_16x16x32_bf16 v[80:83], a[40:43], v[212:215], v[80:83]\n"
      "ds_read_b128 v[232:235], v100 offset:32768\n"
      "v_mfma_f32_16x16x32_bf16 v[136:139], v[212:215], a[44:47], v[136:139]\n"
      "s_add_u32 s82, s82, 4096\n"
      "s_waitcnt lgkmcnt(4)\n"
      "v_mfma_f32_16x16x32_bf16 v[64:67], a[36:39], v[216:219], v[64:67]\n"
      "v_mfma_f32_16x16x32_bf16 v[84:87], a[40:43], v[216:219], v[84:87]\n"
      "ds_read_b128 v[236:239], v100 offset:49152\n"
      "v_mfma_f32_16x16x32_bf16 v[140:143], v[216:219], a[44:47], v[140:143]\n"
      "buffer_load_dwordx4 a[112:115], v5, s[20:23], s82 offen\n"
      "s_waitcnt lgkmcnt(4)\n"
      "v_mfma_f32_16x16x32_bf16 v[68:71], a[36:39], v[220:223], v[68:71]\n"
      "v_mfma_f32_16x16x32_bf16 v[88:91], a[40:43], v[220:223], v[88:91]\n"
      "ds_read_b128 v[240:243], v104\n"
      "v_mfma_f32_16x16x32_bf16 v[144:147], v[220:223], a[44:47], v[144:147]\n"
      "s_waitcnt vmcnt(16)\n"
      "s_waitcnt vmcnt(15)\n"
      "s_waitcnt vmcnt(14)\n"
      "s_waitcnt lgkmcnt(4)\n"
      "v_mfma_f32_16x16x32_bf16 v[72:75], a[52:55], v[224:227], v[72:75]\n"
      "ds_read_b128 v[204:207], v57 offset:256\n"
      "v_mfma_f32_16x16x32_bf16 v[92:95], v[224:227], a[56:59], v[92:95]\n"
      "buffer_load_dwordx4 a[116:119], v5, s[20:23], s82 offen offset:1024\n"
      "s_waitcnt lgkmcnt(4)\n"
      "v_mfma_f32_16x16x32_bf16 v[52:55], a[48:51], v[228:231], v[52:55]\n"
      "v_mfma_f32_16x16x32_bf16 v[76:79], a[52:55], v[228:231], v[76:79]\n"
      "ds_read_b128 v[208:211], v57 offset:16640\n"
      "v_mfma_f32_16x16x32_bf16 v[96:99], v[228:231], a[56:59], v[96:99]\n"
      "buffer_load_dwordx4 a[120:123], v5, s[20:23], s82 offen offset:2048\n"
      "s_waitcnt lgkmcnt(4)\n"
      "v_mfma_f32_16x16x32_bf16 v[60:63], a[48:51], v[232:235], v[60:63]\n"
      "v_mfma_f32_16x16x32_bf16 v[80:83], a[52:55], v[232:235], v[80:83]\n"
      "ds_read_b128 v[212:215], v57 offset:33024\n"
      "v_mfma_f32_16x16x32_bf16 v[136:139], v[232:235], a[56:59], v[136:139]\n"
      "buffer_load_dwordx4 a[124:127], v5, s[20:23], s82 offen offset:3072\n"
      "s_waitcnt lgkmcnt(4)\n"
      "v_mfma_f32_16x16x32_bf16 v[64:67], a[48:51], v[236:239], v[64:67]\n"
      "v_mfma_f32_16x16x32_bf16 v[84:87], a[52:55], v[236:239], v[84:87]\n"
      "ds_read_b128 v[216:219], v57 offset:49408\n"
      "v_mfma_f32_16x16x32_bf16 v[140:143], v[236:239], a[56:59], v[140:143]\n"
      "s_add_u32 s82, s82, 4096\n"
      "s_waitcnt lgkmcnt(4)\n"
      "v_mfma_f32_16x16x32_bf16 v[68:71], a[48:51], v[240:243], v[68:71]\n"
      "v_mfma_f32_16x16x32_bf16 v[88:91], a[52:55], v[240:243], v[88:91]\n"
      "ds_read_b128 v[220:223], v101 offset:256\n"
      "v_mfma_f32_16x16x32_bf16 v[144:147], v[240:243], a[56:59], v[144:147]\n"
      "s_waitcnt vmcnt(16)\n"
      "s_waitcnt vmcnt(15)\n"
      "s_waitcnt vmcnt(14)\n"
      "s_waitcnt lgkmcnt(4)\n"
      "v_mfma_f32_16x16x32_bf16 v[72:75], a[64:67], v[204:207], v[72:75]\n"
      "ds_read_b128 v[224:227], v58 offset:256\n"
      "v_mfma_f32_16x16x32_bf16 v[92:95], v[204:207], a[68:71], v[92:95]\n"
      "buffer_load_dwordx4 a[128:131], v5, s[20:23], s82 offen\n"
      "s_waitcnt lgkmcnt(4)\n"
      "v_mfma_f32_16x16x32_bf16 v[52:55], a[60:63], v[208:211], v[52:55]\n"
      "v_mfma_f32_16x16x32_bf16 v[76:79], a[64:67], v[208:211], v[76:79]\n"
      "ds_read_b128 v[228:231], v58 offset:16640\n"
      "v_mfma_f32_16x16x32_bf16 v[96:99], v[208:211], a[68:71], v[96:99]\n"
      "buffer_load_dwordx4 a[132:135], v5, s[20:23], s82 offen offset:1024\n"
      "s_waitcnt lgkmcnt(4)\n"
      "v_mfma_f32_16x16x32_bf16 v[60:63], a[60:63], v[212:215], v[60:63]\n"
      "v_mfma_f32_16x16x32_bf16 v[80:83], a[64:67], v[212:215], v[80:83]\n"
      "ds_read_b128 v[232:235], v58 offset:33024\n"
      "v_mfma_f32_16x16x32_bf16 v[136:139], v[212:215], a[68:71], v[136:139]\n"
      "buffer_load_dwordx4 a[136:139], v5, s[20:23], s82 offen offset:2048\n"
      "s_waitcnt lgkmcnt(4)\n"
      "v_mfma_f32_16x16x32_bf16 v[64:67], a[60:63], v[216:219], v[64:67]\n"
      "v_mfma_f32_16x16x32_bf16 v[84:87], a[64:67], v[216:219], v[84:87]\n"
      "ds_read_b128 v[236:239], v58 offset:49408\n"
      "v_mfma_f32_16x16x32_bf16 v[140:143], v[216:219], a[68:71], v[140:143]\n"
      "buffer_load_dwordx4 a[140:143], v5, s[20:23], s82 offen offset:3072\n"
      "s_waitcnt lgkmcnt(4)\n"
      "v_mfma_f32_16x16x32_bf16 v[68:71], a[60:63], v[220:223], v[68:71]\n"
      "v_mfma_f32_16x16x32_bf16 v[88:91], a[64:67], v[220:223], v[88:91]\n"
      "ds_read_b128 v[240:243], v102 offset:256\n"
      "v_mfma_f32_16x16x32_bf16 v[144:147], v[220:223], a[68:71], v[144:147]\n"
      "s_waitcnt vmcnt(17)\n"
      "s_waitcnt vmcnt(16)\n"
      "s_waitcnt vmcnt(15)\n"
      "s_waitcnt lgkmcnt(4)\n"
      "v_mfma_f32_16x16x32_bf16 v[72:75], a[76:79], v[224:227], v[72:75]\n"
      "ds_read_b128 v[204:207], v59 offset:256\n"
      "v_mfma_f32_16x16x32_bf16 v[92:95], v[224:227], a[80:83], v[92:95]\n"
      "s_add_u32 s82, s82, 4096\n"
      "s_waitcnt lgkmcnt(4)\n"
      "v_mfma_f32_16x16x32_bf16 v[52:55], a[72:75], v[228:231], v[52:55]\n"
      "v_mfma_f32_16x16x32_bf16 v[76:79], a[76:79], v[228:231], v[76:79]\n"
      "ds_read_b128 v[208:211], v59 offset:16640\n"
      "v_mfma_f32_16x16x32_bf16 v[96:99], v[228:231], a[80:83], v[96:99]\n"
      "buffer_load_dwordx4 a[144:147], v5, s[20:23], s82 offen\n"
      "s_waitcnt lgkmcnt(4)\n"
      "v_mfma_f32_16x16x32_bf16 v[60:63], a[72:75], v[232:235], v[60:63]\n"
      "v_mfma_f32_16x16x32_bf16 v[80:83], a[76:79], v[232:235], v[80:83]\n"
      "ds_read_b128 v[212:215], v59 offset:33024\n"
      "v_mfma_f32_16x16x32_bf16 v[136:139], v[232:235], a[80:83], v[136:139]\n"
      "buffer_load_dwordx4 a[148:151], v5, s[20:23], s82 offen offset:1024\n"
      "s_waitcnt lgkmcnt(4)\n"
      "v_mfma_f32_16x16x32_bf16 v[64:67], a[72:75], v[236:239], v[64:67]\n"
      "v_mfma_f32_16x16x32_bf16 v[84:87], a[76:79], v[236:239], v[84:87]\n"
      "ds_read_b128 v[216:219], v59 offset:49408\n"
      "v_mfma_f32_16x16x32_bf16 v[140:143], v[236:239], a[80:83], v[140:143]\n"
      "buffer_load_dwordx4 a[152:155], v5, s[20:23], s82 offen offset:2048\n"
      "s_waitcnt lgkmcnt(4)\n"
      "v_mfma_f32_16x16x32_bf16 v[68:71], a[72:75], v[240:243], v[68:71]\n"
      "v_mfma_f32_16x16x32_bf16 v[88:91], a[76:79], v[240:243], v[88:91]\n"
      "ds_read_b128 v[220:223], v103 offset:256\n"
      "v_mfma_f32_16x16x32_bf16 v[144:147], v[240:243], a[80:83], v[144:147]\n"
      "s_waitcnt vmcnt(17)\n"
      "s_waitcnt vmcnt(16)\n"
      "s_waitcnt vmcnt(15)\n"
      "s_waitcnt lgkmcnt(4)\n"
      "v_mfma_f32_16x16x32_bf16 v[72:75], a[88:91], v[204:207], v[72:75]\n"
      "ds_read_b128 v[224:227], v100 offset:256\n"
      "v_mfma_f32_16x16x32_bf16 v[92:95], v[204:207], a[92:95], v[92:95]\n"
      "buffer_load_dwordx4 a[156:159], v5, s[20:23], s82 offen offset:3072\n"
      "s_waitcnt lgkmcnt(4)\n"
      "v_mfma_f32_16x16x32_bf16 v[52:55], a[84:87], v[208:211], v[52:55]\n"
      "v_mfma_f32_16x16x32_bf16 v[76:79], a[88:91], v[208:211], v[76:79]\n"
      "ds_read_b128 v[228:231], v100 offset:16640\n"
      "v_mfma_f32_16x16x32_bf16 v[96:99], v[208:211], a[92:95], v[96:99]\n"
      "s_add_u32 s82, s82, 4096\n"
      "s_waitcnt lgkmcnt(4)\n"
      "v_mfma_f32_16x16x32_bf16 v[60:63], a[84:87], v[212:215], v[60:63]\n"
      "v_mfma_f32_16x16x32_bf16 v[80:83], a[88:91], v[212:215], v[80:83]\n"
      "ds_read_b128 v[232:235], v100 offset:33024\n"
      "v_mfma_f32_16x16x32_bf16 v[136:139], v[212:215], a[92:95], v[136:139]\n"
      "buffer_load_dwordx4 a[160:163], v5, s[20:23], s82 offen\n"
      "s_waitcnt lgkmcnt(4)\n"
      "v_mfma_f32_16x16x32_bf16 v[64:67], a[84:87], v[216:219], v[64:67]\n"
      "v_mfma_f32_16x16x32_bf16 v[84:87], a[88:91], v[216:219], v[84:87]\n"
      "ds_read_b128 v[236:239], v100 offset:49408\n"
      "v_mfma_f32_16x16x32_bf16 v[140:143], v[216:219], a[92:95], v[140:143]\n"
      "buffer_load_dwordx4 a[164:167], v5, s[20:23], s82 offen offset:1024\n"
      "s_waitcnt lgkmcnt(4)\n"
      "v_mfma_f32_16x16x32_bf16 v[68:71], a[84:87], v[220:223], v[68:71]\n"
      "v_mfma_f32_16x16x32_bf16 v[88:91], a[88:91], v[220:223], v[88:91]\n"
      "ds_read_b128 v[240:243], v104 offset:256\n"
      "v_mfma_f32_16x16x32_bf16 v[144:147], v[220:223], a[92:95], v[144:147]\n"
      "s_waitcnt vmcnt(17)\n"
      "s_waitcnt vmcnt(16)\n"
      "s_waitcnt vmcnt(15)\n"
      "s_waitcnt lgkmcnt(4)\n"
      "v_mfma_f32_16x16x32_bf16 v[72:75], a[100:103], v[224:227], v[72:75]\n"
      "ds_read_b128 v[204:207], v57 offset:512\n"
      "v_mfma_f32_16x16x32_bf16 v[92:95], v[224:227], a[104:107], v[92:95]\n"
      "buffer_load_dwordx4 a[168:171], v5, s[20:23], s82 offen offset:2048\n"
      "s_waitcnt lgkmcnt(4)\n"
      "v_mfma_f32_16x16x32_bf16 v[52:55], a[96:99], v[228:231], v[52:55]\n"
      "v_mfma_f32_16x16x32_bf16 v[76:79], a[100:103], v[228:231], v[76:79]\n"
      "ds_read_b128 v[208:211], v57 offset:16896\n"
      "v_mfma_f32_16x16x32_bf16 v[96:99], v[228:231], a[104:107], v[96:99]\n"
      "buffer_load_dwordx4 a[172:175], v5, s[20:23], s82 offen offset:3072\n"
      "s_waitcnt lgkmcnt(4)\n"
      "v_mfma_f32_16x16x32_bf16 v[60:63], a[96:99], v[232:235], v[60:63]\n"
      "v_mfma_f32_16x16x32_bf16 v[80:83], a[100:103], v[232:235], v[80:83]\n"
      "ds_read_b128 v[212:215], v57 offset:33280\n"
      "v_mfma_f32_16x16x32_bf16 v[136:139], v[232:235], a[104:107], v[136:139]\n"
      "s_add_u32 s82, s82, 4096\n"
      "s_waitcnt lgkmcnt(4)\n"
      "v_mfma_f32_16x16x32_bf16 v[64:67], a[96:99], v[236:239], v[64:67]\n"
      "v_mfma_f32_16x16x32_bf16 v[84:87], a[100:103], v[236:239], v[84:87]\n"
      "ds_read_b128 v[216:219], v57 offset:49664\n"
      "v_mfma_f32_16x16x32_bf16 v[140:143], v[236:239], a[104:107], v[140:143]\n"
      "buffer_load_dwordx4 a[176:179], v5, s[20:23], s82 offen\n"
      "s_waitcnt lgkmcnt(4)\n"
      "v_mfma_f32_16x16x32_bf16 v[68:71], a[96:99], v[240:243], v[68:71]\n"
      "v_mfma_f32_16x16x32_bf16 v[88:91], a[100:103], v[240:243], v[88:91]\n"
      "ds_read_b128 v[220:223], v101 offset:512\n"
      "v_mfma_f32_16x16x32_bf16 v[144:147], v[240:243], a[104:107], v[144:147]\n"
      "s_waitcnt vmcnt(17)\n"
      "s_waitcnt vmcnt(16)\n"
      "s_waitcnt vmcnt(15)\n"
      "s_waitcnt lgkmcnt(4)\n"
      "v_mfma_f32_16x16x32_bf16 v[72:75], a[112:115], v[204:207], v[72:75]\n"
      "ds_read_b128 v[224:227], v58 offset:512\n"
      "v_mfma_f32_16x16x32_bf16 v[92:95], v[204:207], a[116:119], v[92:95]\n"
      "buffer_load_dwordx4 a[180:183], v5, s[20:23], s82 offen offset:1024\n"
      "s_waitcnt lgkmcnt(4)\n"
      "v_mfma_f32_16x16x32_bf16 v[52:55], a[108:111], v[208:211], v[52:55]\n"
      "v_mfma_f32_16x16x32_bf16 v[76:79], a[112:115], v[208:211], v[76:79]\n"
      "ds_read_b128 v[228:231], v58 offset:16896\n"
      "v_mfma_f32_16x16x32_bf16 v[96:99], v[208:211], a[116:119], v[96:99]\n"
      "buffer_load_dwordx4 a[184:187], v5, s[20:23], s82 offen offset:2048\n"
      "s_waitcnt lgkmcnt(4)\n"
      "v_mfma_f32_16x16x32_bf16 v[60:63], a[108:111], v[212:215], v[60:63]\n"
      "v_mfma_f32_16x16x32_bf16 v[80:83], a[112:115], v[212:215], v[80:83]\n"
      "ds_read_b128 v[232:235], v58 offset:33280\n"
      "v_mfma_f32_16x16x32_bf16 v[136:139], v[212:215], a[116:119], v[136:139]\n"
      "buffer_load_dwordx4 a[188:191], v5, s[20:23], s82 offen offset:3072\n"
      "s_waitcnt lgkmcnt(4)\n"
      "v_mfma_f32_16x16x32_bf16 v[64:67], a[108:111], v[216:219], v[64:67]\n"
      "v_mfma_f32_16x16x32_bf16 v[84:87], a[112:115], v[216:219], v[84:87]\n"
      "ds_read_b128 v[236:239], v58 offset:49664\n"
      "v_mfma_f32_16x16x32_bf16 v[140:143], v[216:219], a[116:119], v[140:143]\n"
      "s_add_u32 s82, s82, 4096\n"
      "s_waitcnt lgkmcnt(4)\n"
      "v_mfma_f32_16x16x32_bf16 v[68:71], a[108:111], v[220:223], v[68:71]\n"
      "v_mfma_f32_16x16x32_bf16 v[88:91], a[112:115], v[220:223], v[88:91]\n"
      "ds_read_b128 v[240:243], v102 offset:512\n"
      "v_mfma_f32_16x16x32_bf16 v[144:147], v[220:223], a[116:119], v[144:147]\n"
      "s_waitcnt vmcnt(17)\n"
      "s_waitcnt vmcnt(16)\n"
      "s_waitcnt vmcnt(15)\n"
      "s_waitcnt lgkmcnt(4)\n"
      "v_mfma_f32_16x16x32_bf16 v[72:75], a[124:127], v[224:227], v[72:75]\n"
      "ds_read_b128 v[204:207], v59 offset:512\n"
      "v_mfma_f32_16x16x32_bf16 v[92:95], v[224:227], a[128:131], v[92:95]\n"
      "buffer_load_dwordx4 a[192:195], v5, s[20:23], s82 offen\n"
      "s_waitcnt lgkmcnt(4)\n"
      "v_mfma_f32_16x16x32_bf16 v[52:55], a[120:123], v[228:231], v[52:55]\n"
      "v_mfma_f32_16x16x32_bf16 v[76:79], a[124:127], v[228:231], v[76:79]\n"
      "ds_read_b128 v[208:211], v59 offset:16896\n"
      "v_mfma_f32_16x16x32_bf16 v[96:99], v[228:231], a[128:131], v[96:99]\n"
      "buffer_load_dwordx4 a[196:199], v5, s[20:23], s82 offen offset:1024\n"
      "s_waitcnt lgkmcnt(4)\n"
      "v_mfma_f32_16x16x32_bf16 v[60:63], a[120:123], v[232:235], v[60:63]\n"
      "v_mfma_f32_16x16x32_bf16 v[80:83], a[124:127], v[232:235], v[80:83]\n"
      "ds_read_b128 v[212:215], v59 offset:33280\n"
      "v_mfma_f32_16x16x32_bf16 v[136:139], v[232:235], a[128:131], v[136:139]\n"
      "buffer_load_dwordx4 a[200:203], v5, s[20:23], s82 offen offset:2048\n"
      "s_waitcnt lgkmcnt(4)\n"
      "v_mfma_f32_16x16x32_bf16 v[64:67], a[120:123], v[236:239], v[64:67]\n"
      "v_mfma_f32_16x16x32_bf16 v[84:87], a[124:127], v[236:239], v[84:87]\n"
      "ds_read_b128 v[216:219], v59 offset:49664\n"
      "v_mfma_f32_16x16x32_bf16 v[140:143], v[236:239], a[128:131], v[140:143]\n"
      "s_waitcnt lgkmcnt(4)\n"
      "v_mfma_f32_16x16x32_bf16 v[68:71], a[120:123], v[240:243], v[68:71]\n"
      "v_mfma_f32_16x16x32_bf16 v[88:91], a[124:127], v[240:243], v[88:91]\n"
      "ds_read_b128 v[220:223], v103 offset:512\n"
      "v_mfma_f32_16x16x32_bf16 v[144:147], v[240:243], a[128:131], v[144:147]\n"
      "s_waitcnt vmcnt(17)\n"
      "s_waitcnt vmcnt(16)\n"
      "s_waitcnt vmcnt(15)\n"
      "s_waitcnt lgkmcnt(4)\n"
      "v_mfma_f32_16x16x32_bf16 v[72:75], a[136:139], v[204:207], v[72:75]\n"
      "ds_read_b128 v[224:227], v100 offset:512\n"
      "v_mfma_f32_16x16x32_bf16 v[92:95], v[204:207], a[140:143], v[92:95]\n"
      "s_waitcnt lgkmcnt(4)\n"
      "v_mfma_f32_16x16x32_bf16 v[52:55], a[132:135], v[208:211], v[52:55]\n"
      "v_mfma_f32_16x16x32_bf16 v[76:79], a[136:139], v[208:211], v[76:79]\n"
      "ds_read_b128 v[228:231], v100 offset:16896\n"
      "v_mfma_f32_16x16x32_bf16 v[96:99], v[208:211], a[140:143], v[96:99]\n"
      "s_waitcnt lgkmcnt(4)\n"
      "v_mfma_f32_16x16x32_bf16 v[60:63], a[132:135], v[212:215], v[60:63]\n"
      "v_mfma_f32_16x16x32_bf16 v[80:83], a[136:139], v[212:215], v[80:83]\n"
      "ds_read_b128 v[232:235], v100 offset:33280\n"
      "v_mfma_f32_16x16x32_bf16 v[136:139], v[212:215], a[140:143], v[136:139]\n"
      "s_waitcnt lgkmcnt(4)\n"
      "v_mfma_f32_16x16x32_bf16 v[64:67], a[132:135], v[216:219], v[64:67]\n"
      "v_mfma_f32_16x16x32_bf16 v[84:87], a[136:139], v[216:219], v[84:87]\n"
      "ds_read_b128 v[236:239], v100 offset:49664\n"
      "v_mfma_f32_16x16x32_bf16 v[140:143], v[216:219], a[140:143], v[140:143]\n"
      "s_waitcnt lgkmcnt(4)\n"
      "v_mfma_f32_16x16x32_bf16 v[68:71], a[132:135], v[220:223], v[68:71]\n"
      "v_mfma_f32_16x16x32_bf16 v[88:91], a[136:139], v[220:223], v[88:91]\n"
      "ds_read_b128 v[240:243], v104 offset:512\n"
      "v_mfma_f32_16x16x32_bf16 v[144:147], v[220:223], a[140:143], v[144:147]\n"
      "s_waitcnt vmcnt(14)\n"
      "s_waitcnt vmcnt(13)\n"
      "s_waitcnt vmcnt(12)\n"
      "s_waitcnt lgkmcnt(4)\n"
      "v_mfma_f32_16x16x32_bf16 v[72:75], a[148:151], v[224:227], v[72:75]\n"
      "ds_read_b128 v[204:207], v57 offset:768\n"
      "v_mfma_f32_16x16x32_bf16 v[92:95], v[224:227], a[152:155], v[92:95]\n"
      "s_waitcnt lgkmcnt(4)\n"
      "v_mfma_f32_16x16x32_bf16 v[52:55], a[144:147], v[228:231], v[52:55]\n"
      "v_mfma_f32_16x16x32_bf16 v[76:79], a[148:151], v[228:231], v[76:79]\n"
      "ds_read_b128 v[208:211], v57 offset:17152\n"
      "v_mfma_f32_16x16x32_bf16 v[96:99], v[228:231], a[152:155], v[96:99]\n"
      "s_waitcnt lgkmcnt(4)\n"
      "v_mfma_f32_16x16x32_bf16 v[60:63], a[144:147], v[232:235], v[60:63]\n"
      "v_mfma_f32_16x16x32_bf16 v[80:83], a[148:151], v[232:235], v[80:83]\n"
      "ds_read_b128 v[212:215], v57 offset:33536\n"
      "v_mfma_f32_16x16x32_bf16 v[136:139], v[232:235], a[152:155], v[136:139]\n"
      "s_waitcnt lgkmcnt(4)\n"
      "v_mfma_f32_16x16x32_bf16 v[64:67], a[144:147], v[236:239], v[64:67]\n"
      "v_mfma_f32_16x16x32_bf16 v[84:87], a[148:151], v[236:239], v[84:87]\n"
      "ds_read_b128 v[216:219], v57 offset:49920\n"
      "v_mfma_f32_16x16x32_bf16 v[140:143], v[236:239], a[152:155], v[140:143]\n"
      "s_waitcnt lgkmcnt(4)\n"
      "v_mfma_f32_16x16x32_bf16 v[68:71], a[144:147], v[240:243], v[68:71]\n"
      "v_mfma_f32_16x16x32_bf16 v[88:91], a[148:151], v[240:243], v[88:91]\n"
      "ds_read_b128 v[220:223], v101 offset:768\n"
      "v_mfma_f32_16x16x32_bf16 v[144:147], v[240:243], a[152:155], v[144:147]\n"
      "v_and_b32 v106, 31, v7\n"
      "v_lshlrev_b32 v106, 2, v106\n"
      "s_lshl_b32 s64, s29, 7\n"
      "s_add_u32 s64, s64, 5313536\n"
      "buffer_load_dword v107, v106, s[20:23], s64 offen sc1\n"
      "s_waitcnt vmcnt(12)\n"
      "s_waitcnt vmcnt(11)\n"
      "s_waitcnt vmcnt(10)\n"
      "s_waitcnt lgkmcnt(4)\n"
      "v_mfma_f32_16x16x32_bf16 v[72:75], a[160:163], v[204:207], v[72:75]\n"
      "ds_read_b128 v[224:227], v58 offset:768\n"
      "v_mfma_f32_16x16x32_bf16 v[92:95], v[204:207], a[164:167], v[92:95]\n"
      "s_waitcnt lgkmcnt(4)\n"
      "v_mfma_f32_16x16x32_bf16 v[52:55], a[156:159], v[208:211], v[52:55]\n"
      "v_mfma_f32_16x16x32_bf16 v[76:79], a[160:163], v[208:211], v[76:79]\n"
      "ds_read_b128 v[228:231], v58 offset:17152\n"
      "v_mfma_f32_16x16x32_bf16 v[96:99], v[208:211], a[164:167], v[96:99]\n"
      "s_waitcnt lgkmcnt(4)\n"
      "v_mfma_f32_16x16x32_bf16 v[60:63], a[156:159], v[212:215], v[60:63]\n"
      "v_mfma_f32_16x16x32_bf16 v[80:83], a[160:163], v[212:215], v[80:83]\n"
      "ds_read_b128 v[232:235], v58 offset:33536\n"
      "v_mfma_f32_16x16x32_bf16 v[136:139], v[212:215], a[164:167], v[136:139]\n"
      "s_waitcnt lgkmcnt(4)\n"
      "v_mfma_f32_16x16x32_bf16 v[64:67], a[156:159], v[216:219], v[64:67]\n"
      "v_mfma_f32_16x16x32_bf16 v[84:87], a[160:163], v[216:219], v[84:87]\n"
      "ds_read_b128 v[236:239], v58 offset:49920\n"
      "v_mfma_f32_16x16x32_bf16 v[140:143], v[216:219], a[164:167], v[140:143]\n"
      "s_waitcnt lgkmcnt(4)\n"
      "v_mfma_f32_16x16x32_bf16 v[68:71], a[156:159], v[220:223], v[68:71]\n"
      "v_mfma_f32_16x16x32_bf16 v[88:91], a[160:163], v[220:223], v[88:91]\n"
      "ds_read_b128 v[240:243], v102 offset:768\n"
      "v_mfma_f32_16x16x32_bf16 v[144:147], v[220:223], a[164:167], v[144:147]\n"
      "s_waitcnt vmcnt(9)\n"
      "s_waitcnt vmcnt(8)\n"
      "s_waitcnt vmcnt(7)\n"
      "s_waitcnt lgkmcnt(4)\n"
      "v_mfma_f32_16x16x32_bf16 v[72:75], a[172:175], v[224:227], v[72:75]\n"
      "ds_read_b128 v[204:207], v59 offset:768\n"
      "v_mfma_f32_16x16x32_bf16 v[92:95], v[224:227], a[176:179], v[92:95]\n"
      "s_waitcnt lgkmcnt(4)\n"
      "v_mfma_f32_16x16x32_bf16 v[52:55], a[168:171], v[228:231], v[52:55]\n"
      "v_mfma_f32_16x16x32_bf16 v[76:79], a[172:175], v[228:231], v[76:79]\n"
      "ds_read_b128 v[208:211], v59 offset:17152\n"
      "v_mfma_f32_16x16x32_bf16 v[96:99], v[228:231], a[176:179], v[96:99]\n"
      "s_waitcnt lgkmcnt(4)\n"
      "v_mfma_f32_16x16x32_bf16 v[60:63], a[168:171], v[232:235], v[60:63]\n"
      "v_mfma_f32_16x16x32_bf16 v[80:83], a[172:175], v[232:235], v[80:83]\n"
      "ds_read_b128 v[212:215], v59 offset:33536\n"
      "v_mfma_f32_16x16x32_bf16 v[136:139], v[232:235], a[176:179], v[136:139]\n"
      "s_waitcnt lgkmcnt(4)\n"
      "v_mfma_f32_16x16x32_bf16 v[64:67], a[168:171], v[236:239], v[64:67]\n"
      "v_mfma_f32_16x16x32_bf16 v[84:87], a[172:175], v[236:239], v[84:87]\n"
      "ds_read_b128 v[216:219], v59 offset:49920\n"
      "v_mfma_f32_16x16x32_bf16 v[140:143], v[236:239], a[176:179], v[140:143]\n"
      "s_waitcnt lgkmcnt(4)\n"
      "v_mfma_f32_16x16x32_bf16 v[68:71], a[168:171], v[240:243], v[68:71]\n"
      "v_mfma_f32_16x16x32_bf16 v[88:91], a[172:175], v[240:243], v[88:91]\n"
      "ds_read_b128 v[220:223], v103 offset:768\n"
      "v_mfma_f32_16x16x32_bf16 v[144:147], v[240:243], a[176:179], v[144:147]\n"
      "s_waitcnt vmcnt(6)\n"
      "s_waitcnt vmcnt(5)\n"
      "s_waitcnt vmcnt(4)\n"
      "s_waitcnt lgkmcnt(4)\n"
      "v_mfma_f32_16x16x32_bf16 v[72:75], a[184:187], v[204:207], v[72:75]\n"
      "ds_read_b128 v[224:227], v100 offset:768\n"
      "v_mfma_f32_16x16x32_bf16 v[92:95], v[204:207], a[188:191], v[92:95]\n"
      "s_waitcnt lgkmcnt(4)\n"
      "v_mfma_f32_16x16x32_bf16 v[52:55], a[180:183], v[208:211], v[52:55]\n"
      "v_mfma_f32_16x16x32_bf16 v[76:79], a[184:187], v[208:211], v[76:79]\n"
      "ds_read_b128 v[228:231], v100 offset:17152\n"
      "v_mfma_f32_16x16x32_bf16 v[96:99], v[208:211], a[188:191], v[96:99]\n"
      "s_waitcnt lgkmcnt(4)\n"
      "v_mfma_f32_16x16x32_bf16 v[60:63], a[180:183], v[212:215], v[60:63]\n"
      "v_mfma_f32_16x16x32_bf16 v[80:83], a[184:187], v[212:215], v[80:83]\n"
      "ds_read_b128 v[232:235], v100 offset:33536\n"
      "v_mfma_f32_16x16x32_bf16 v[136:139], v[212:215], a[188:191], v[136:139]\n"
      "s_waitcnt lgkmcnt(4)\n"
      "v_mfma_f32_16x16x32_bf16 v[64:67], a[180:183], v[216:219], v[64:67]\n"
      "v_mfma_f32_16x16x32_bf16 v[84:87], a[184:187], v[216:219], v[84:87]\n"
      "ds_read_b128 v[236:239], v100 offset:49920\n"
      "v_mfma_f32_16x16x32_bf16 v[140:143], v[216:219], a[188:191], v[140:143]\n"
      "s_waitcnt lgkmcnt(4)\n"
      "v_mfma_f32_16x16x32_bf16 v[68:71], a[180:183], v[220:223], v[68:71]\n"
      "v_mfma_f32_16x16x32_bf16 v[88:91], a[184:187], v[220:223], v[88:91]\n"
      "ds_read_b128 v[240:243], v104 offset:768\n"
      "v_mfma_f32_16x16x32_bf16 v[144:147], v[220:223], a[188:191], v[144:147]\n"
      "s_waitcnt vmcnt(3)\n"
      "s_waitcnt vmcnt(2)\n"
      "s_waitcnt vmcnt(1)\n"
      "s_waitcnt lgkmcnt(4)\n"
      "v_mfma_f32_16x16x32_bf16 v[72:75], a[196:199], v[224:227], v[72:75]\n"
      "v_mfma_f32_16x16x32_bf16 v[92:95], v[224:227], a[200:203], v[92:95]\n"
      "s_waitcnt lgkmcnt(3)\n"
      "v_mfma_f32_16x16x32_bf16 v[52:55], a[192:195], v[228:231], v[52:55]\n"
      "v_mfma_f32_16x16x32_bf16 v[76:79], a[196:199], v[228:231], v[76:79]\n"
      "v_mfma_f32_16x16x32_bf16 v[96:99], v[228:231], a[200:203], v[96:99]\n"
      "s_waitcnt lgkmcnt(2)\n"
      "v_mfma_f32_16x16x32_bf16 v[60:63], a[192:195], v[232:235], v[60:63]\n"
      "v_mfma_f32_16x16x32_bf16 v[80:83], a[196:199], v[232:235], v[80:83]\n"
      "v_mfma_f32_16x16x32_bf16 v[136:139], v[232:235], a[200:203], v[136:139]\n"
      "s_waitcnt lgkmcnt(1)\n"
      "v_mfma_f32_16x16x32_bf16 v[64:67], a[192:195], v[236:239], v[64:67]\n"
      "v_mfma_f32_16x16x32_bf16 v[84:87], a[196:199], v[236:239], v[84:87]\n"
      "v_mfma_f32_16x16x32_bf16 v[140:143], v[236:239], a[200:203], v[140:143]\n"
      "s_waitcnt lgkmcnt(0)\n"
      "v_mfma_f32_16x16x32_bf16 v[68:71], a[192:195], v[240:243], v[68:71]\n"
      "v_mfma_f32_16x16x32_bf16 v[88:91], a[196:199], v[240:243], v[88:91]\n"
      "v_mfma_f32_16x16x32_bf16 v[144:147], v[240:243], a[200:203], v[144:147]\n"
      "; P1 epilogue\n"
      "s_waitcnt vmcnt(0)\n"
      "s_mov_b32 s86, 0\n"
      "Lfbc_%=:\n"
      "v_cmp_ne_u32 vcc, s31, v107\n"
      "s_nop 4\n"
      "s_cbranch_vccz Lfbd_%=\n"
      "s_add_u32 s86, s86, 1\n"
      "s_cmp_gt_u32 s86, 0x10000\n"
      "s_cbranch_scc1 Lfbd_%=\n"
      "buffer_load_dword v107, v106, s[20:23], s64 offen sc1\n"
      "s_waitcnt vmcnt(0)\n"
      "s_branch Lfbc_%=\n"
      "s_nop 0\n"
      "Lfbd_%=:\n"
      "s_lshl_b32 s83, s29, 8\n"
      "s_lshl_b32 s84, s30, 6\n"
      "s_add_u32 s83, s83, s84\n"
      "s_add_u32 s83, s83, 5275648\n"
      "v_lshlrev_b32 v50, 4, v49\n"
      "buffer_load_dwordx4 v[120:123], v50, s[20:23], s83 offen\n"
      "buffer_load_dwordx4 v[124:127], v50, s[20:23], s83 offen offset:2048\n"
      "ds_read_b128 v[100:103], v105 offset:320\n"
      "ds_read_b128 v[148:151], v105 offset:384\n"
      "ds_read_b128 v[152:155], v105 offset:448\n"
      "ds_read_b128 v[164:167], v105 offset:512\n"
      "ds_read_b128 v[168:171], v105 offset:576\n"
      "v_mul_f32 v57, 0x3eb8aa3b, v157\n"
      "v_mul_f32 v58, 0x3eb8aa3b, v158\n"
      "v_mul_f32 v59, 0x3eb8aa3b, v159\n"
      "v_mul_f32 v104, 0x3eb8aa3b, v160\n"
      "v_pk_mul_f32 v[72:73], v[72:73], v[156:157] op_sel:[0,0] op_sel_hi:[1,0]\n"
      "v_pk_mul_f32 v[74:75], v[74:75], v[156:157] op_sel:[0,0] op_sel_hi:[1,0]\n"
      "v_pk_mul_f32 v[76:77], v[76:77], v[156:157] op_sel:[0,1] op_sel_hi:[1,1]\n"
      "v_pk_mul_f32 v[78:79], v[78:79], v[156:157] op_sel:[0,1] op_sel_hi:[1,1]\n"
      "v_pk_mul_f32 v[52:53], v[52:53], v[56:57] op_sel:[0,1] op_sel_hi:[1,1]\n"
      "v_pk_mul_f32 v[54:55], v[54:55], v[56:57] op_sel:[0,1] op_sel_hi:[1,1]\n"
      "v_pk_mul_f32 v[80:81], v[80:81], v[158:159] op_sel:[0,0] op_sel_hi:[1,0]\n"
      "v_pk_mul_f32 v[82:83], v[82:83], v[158:159] op_sel:[0,0] op_sel_hi:[1,0]\n"
      "v_pk_mul_f32 v[60:61], v[60:61], v[58:59] op_sel:[0,0] op_sel_hi:[1,0]\n"
      "v_pk_mul_f32 v[62:63], v[62:63], v[58:59] op_sel:[0,0] op_sel_hi:[1,0]\n"
      "buffer_load_dwordx4 a[204:207], v5, s[20:23], s82 offen offset:3072\n"
      "v_pk_mul_f32 v[84:85], v[84:85], v[158:159] op_sel:[0,1] op_sel_hi:[1,1]\n"
      "v_pk_mul_f32 v[86:87], v[86:87], v[158:159] op_sel:[0,1] op_sel_hi:[1,1]\n"
      "v_pk_mul_f32 v[64:65], v[64:65], v[58:59] op_sel:[0,1] op_sel_hi:[1,1]\n"
      "v_pk_mul_f32 v[66:67], v[66:67], v[58:59] op_sel:[0,1] op_sel_hi:[1,1]\n"
      "v_pk_mul_f32 v[88:89], v[88:89], v[160:161] op_sel:[0,0] op_sel_hi:[1,0]\n"
      "v_pk_mul_f32 v[90:91], v[90:91], v[160:161] op_sel:[0,0] op_sel_hi:[1,0]\n"
      "v_pk_mul_f32 v[68:69], v[68:69], v[104:105] op_sel:[0,0] op_sel_hi:[1,0]\n"
      "v_pk_mul_f32 v[70:71], v[70:71], v[104:105] op_sel:[0,0] op_sel_hi:[1,0]\n"
      "v_cvt_pk_bf16_f32 v176, v72, v73\n"
      "v_cvt_pk_bf16_f32 v177, v74, v75\n"
      "v_cvt_pk_bf16_f32 v178, v76, v77\n"
      "v_cvt_pk_bf16_f32 v179, v78, v79\n"
      "v_cvt_pk_bf16_f32 v106, v52, v53\n"
      "v_cvt_pk_bf16_f32 v107, v54, v55\n"
      "v_cvt_pk_bf16_f32 v180, v80, v81\n"
      "v_cvt_pk_bf16_f32 v181, v82, v83\n"
      "v_cvt_pk_bf16_f32 v162, v60, v61\n"
      "v_cvt_pk_bf16_f32 v163, v62, v63\n"
      "s_add_u32 s82, s82, 4096\n"
      "buffer_load_dwordx4 a[208:211], v5, s[20:23], s82 offen\n"
      "v_cvt_pk_bf16_f32 v182, v84, v85\n"
      "v_cvt_pk_bf16_f32 v183, v86, v87\n"
      "v_cvt_pk_bf16_f32 v172, v64, v65\n"
      "v_cvt_pk_bf16_f32 v173, v66, v67\n"
      "v_cvt_pk_bf16_f32 v184, v88, v89\n"
      "v_cvt_pk_bf16_f32 v185, v90, v91\n"
      "v_cvt_pk_bf16_f32 v174, v68, v69\n"
      "v_cvt_pk_bf16_f32 v175, v70, v71\n"
      "v_mfma_f32_16x16x16_bf16 v[196:199], v[176:177], v[106:107], v[116:119]\n"
      "v_mfma_f32_16x16x16_bf16 v[212:215], v[178:179], v[106:107], v[108:111]\n"
      "v_mfma_f32_16x16x16_bf16 v[200:203], v[178:179], v[162:163], v[112:115]\n"
      "v_mfma_f32_16x16x16_bf16 v[216:219], v[180:181], v[162:163], v[108:111]\n"
      "buffer_load_dwordx4 a[212:215], v5, s[20:23], s82 offen offset:1024\n"
      "v_mfma_f32_16x16x16_bf16 v[204:207], v[180:181], v[172:173], v[112:115]\n"
      "v_mfma_f32_16x16x16_bf16 v[220:223], v[182:183], v[172:173], v[108:111]\n"
      "v_mfma_f32_16x16x16_bf16 v[208:211], v[182:183], v[174:175], v[112:115]\n"
      "v_mfma_f32_16x16x16_bf16 v[224:227], v[184:185], v[174:175], v[108:111]\n"
      "s_waitcnt lgkmcnt(0)\n"
      "v_pk_mul_f32 v[92:93], v[92:93], v[100:101] op_sel:[0,0] op_sel_hi:[1,1]\n"
      "v_pk_mul_f32 v[94:95], v[94:95], v[102:103] op_sel:[0,0] op_sel_hi:[1,1]\n"
      "v_pk_mul_f32 v[96:97], v[96:97], v[148:149] op_sel:[0,0] op_sel_hi:[1,1]\n"
      "v_pk_mul_f32 v[98:99], v[98:99], v[150:151] op_sel:[0,0] op_sel_hi:[1,1]\n"
      "v_pk_mul_f32 v[136:137], v[136:137], v[152:153] op_sel:[0,0] op_sel_hi:[1,1]\n"
      "buffer_load_dwordx4 a[216:219], v5, s[20:23], s82 offen offset:2048\n"
      "v_pk_mul_f32 v[138:139], v[138:139], v[154:155] op_sel:[0,0] op_sel_hi:[1,1]\n"
      "v_pk_mul_f32 v[140:141], v[140:141], v[164:165] op_sel:[0,0] op_sel_hi:[1,1]\n"
      "v_pk_mul_f32 v[142:143], v[142:143], v[166:167] op_sel:[0,0] op_sel_hi:[1,1]\n"
      "v_pk_mul_f32 v[144:145], v[144:145], v[168:169] op_sel:[0,0] op_sel_hi:[1,1]\n"
      "v_pk_mul_f32 v[146:147], v[146:147], v[170:171] op_sel:[0,0] op_sel_hi:[1,1]\n"
      "v_cvt_pk_bf16_f32 v186, v92, v93\n"
      "v_cvt_pk_bf16_f32 v187, v94, v95\n"
      "v_cvt_pk_bf16_f32 v188, v96, v97\n"
      "v_cvt_pk_bf16_f32 v189, v98, v99\n"
      "v_cvt_pk_bf16_f32 v190, v136, v137\n"
      "v_cvt_pk_bf16_f32 v191, v138, v139\n"
      "v_cvt_pk_bf16_f32 v192, v140, v141\n"
      "v_cvt_pk_bf16_f32 v193, v142, v143\n"
      "v_cvt_pk_bf16_f32 v194, v144, v145\n"
      "v_cvt_pk_bf16_f32 v195, v146, v147\n"
      "v_lshlrev_b32 v50, 2, v49\n"
      "v_add_u32 v51, 6, v48\n"
      "v_cmp_ge_u32 s[64:65], v50, v51\n"
      "buffer_load_dwordx4 a[220:223], v5, s[20:23], s82 offen offset:3072\n"
      "v_max_f32 v196, v196, v212\n"
      "v_max_f32 v197, v197, v213\n"
      "v_max_f32 v198, v198, v214\n"
      "v_max_f32 v199, v199, v215\n"
      "v_max_f32 v200, v200, v216\n"
      "v_max_f32 v201, v201, v217\n"
      "v_max_f32 v202, v202, v218\n"
      "v_max_f32 v203, v203, v219\n"
      "v_max_f32 v204, v204, v220\n"
      "v_max_f32 v205, v205, v221\n"
      "v_max_f32 v206, v206, v222\n"
      "v_max_f32 v207, v207, v223\n"
      "v_max_f32 v208, v208, v224\n"
      "v_max_f32 v209, v209, v225\n"
      "v_max_f32 v210, v210, v226\n"
      "v_max_f32 v211, v211, v227\n"
      "v_max3_f32 v52, v196, v197, v198\n"
      "v_max3_f32 v53, v200, v201, v202\n"
      "s_add_u32 s82, s82, 4096\n"
      "buffer_load_dwordx4 a[224:227], v5, s[20:23], s82 offen\n"
      "v_max3_f32 v54, v204, v205, v206\n"
      "v_max3_f32 v55, v208, v209, v210\n"
      "v_max_f32 v52, v52, v199\n"
      "v_max_f32 v53, v53, v203\n"
      "v_max_f32 v54, v54, v207\n"
      "v_max_f32 v55, v55, v211\n"
      "v_mov_b32 v56, v52\n"
      "v_mov_b32 v57, v53\n"
      "v_mov_b32 v58, v54\n"
      "v_mov_b32 v59, v55\n"
      "v_permlane16_swap_b32 v52, v56\n"
      "v_permlane16_swap_b32 v53, v57\n"
      "v_permlane16_swap_b32 v54, v58\n"
      "v_permlane16_swap_b32 v55, v59\n"
      "v_max_f32 v52, v52, v56\n"
      "buffer_load_dwordx4 a[228:231], v5, s[20:23], s82 offen offset:1024\n"
      "v_max_f32 v53, v53, v57\n"
      "v_max_f32 v54, v54, v58\n"
      "v_max_f32 v55, v55, v59\n"
      "v_mov_b32 v56, v52\n"
      "v_mov_b32 v57, v53\n"
      "v_mov_b32 v58, v54\n"
      "v_mov_b32 v59, v55\n"
      "v_permlane32_swap_b32 v52, v56\n"
      "v_permlane32_swap_b32 v53, v57\n"
      "v_permlane32_swap_b32 v54, v58\n"
      "v_permlane32_swap_b32 v55, v59\n"
      "v_max_f32 v52, v52, v56\n"
      "v_max_f32 v53, v53, v57\n"
      "v_max_f32 v54, v54, v58\n"
      "v_max_f32 v55, v55, v59\n"
      "buffer_load_dwordx4 a[232:235], v5, s[20:23], s82 offen offset:2048\n"
      "v_pk_add_f32 v[196:197], v[196:197], v[52:53] op_sel:[0,0] op_sel_hi:[1,0] neg_lo:[0,1] neg_hi:[0,1]\n"
      "v_pk_add_f32 v[198:199], v[198:199], v[52:53] op_sel:[0,0] op_sel_hi:[1,0] neg_lo:[0,1] neg_hi:[0,1]\n"
      "v_pk_add_f32 v[200:201], v[200:201], v[52:53] op_sel:[0,1] op_sel_hi:[1,1] neg_lo:[0,1] neg_hi:[0,1]\n"
      "v_pk_add_f32 v[202:203], v[202:203], v[52:53] op_sel:[0,1] op_sel_hi:[1,1] neg_lo:[0,1] neg_hi:[0,1]\n"
      "v_pk_add_f32 v[204:205], v[204:205], v[54:55] op_sel:[0,0] op_sel_hi:[1,0] neg_lo:[0,1] neg_hi:[0,1]\n"
      "v_pk_add_f32 v[206:207], v[206:207], v[54:55] op_sel:[0,0] op_sel_hi:[1,0] neg_lo:[0,1] neg_hi:[0,1]\n"
      "v_pk_add_f32 v[208:209], v[208:209], v[54:55] op_sel:[0,1] op_sel_hi:[1,1] neg_lo:[0,1] neg_hi:[0,1]\n"
      "v_pk_add_f32 v[210:211], v[210:211], v[54:55] op_sel:[0,1] op_sel_hi:[1,1] neg_lo:[0,1] neg_hi:[0,1]\n"
      "v_exp_f32 v196, v196\n"
      "v_exp_f32 v197, v197\n"
      "v_exp_f32 v198, v198\n"
      "v_exp_f32 v199, v199\n"
      "v_exp_f32 v200, v200\n"
      "v_exp_f32 v201, v201\n"
      "buffer_load_dwordx4 a[236:239], v5, s[20:23], s82 offen offset:3072\n"
      "v_exp_f32 v202, v202\n"
      "v_exp_f32 v203, v203\n"
      "v_exp_f32 v204, v204\n"
      "v_exp_f32 v205, v205\n"
      "v_exp_f32 v206, v206\n"
      "v_exp_f32 v207, v207\n"
      "v_exp_f32 v208, v208\n"
      "v_exp_f32 v209, v209\n"
      "v_exp_f32 v210, v210\n"
      "v_exp_f32 v211, v211\n"
      "s_add_u32 s82, s82, 4096\n"
      "buffer_load_dwordx4 a[240:243], v5, s[20:23], s82 offen\n"
      "v_cvt_pk_bf16_f32 v60, v196, v197\n"
      "v_cvt_pk_bf16_f32 v61, v198, v199\n"
      "v_cvt_pk_bf16_f32 v62, v200, v201\n"
      "v_cvt_pk_bf16_f32 v63, v202, v203\n"
      "v_cvt_pk_bf16_f32 v64, v204, v205\n"
      "v_cvt_pk_bf16_f32 v65, v206, v207\n"
      "v_cvt_pk_bf16_f32 v66, v208, v209\n"
      "v_cvt_pk_bf16_f32 v67, v210, v211\n"
      "v_cndmask_b32 v68, 0, v60, s[64:65]\n"
      "v_cndmask_b32 v76, v60, 0, s[64:65]\n"
      "v_cndmask_b32 v69, 0, v61, s[64:65]\n"
      "v_cndmask_b32 v77, v61, 0, s[64:65]\n"
      "v_cndmask_b32 v70, 0, v62, s[64:65]\n"
      "v_cndmask_b32 v78, v62, 0, s[64:65]\n"
      "v_cndmask_b32 v71, 0, v63, s[64:65]\n"
      "v_cndmask_b32 v79, v63, 0, s[64:65]\n"
      "v_cndmask_b32 v72, 0, v64, s[64:65]\n"
      "v_cndmask_b32 v80, v64, 0, s[64:65]\n"
      "buffer_load_dwordx4 a[244:247], v5, s[20:23], s82 offen offset:1024\n"
      "v_cndmask_b32 v73, 0, v65, s[64:65]\n"
      "v_cndmask_b32 v81, v65, 0, s[64:65]\n"
      "v_cndmask_b32 v74, 0, v66, s[64:65]\n"
      "v_cndmask_b32 v82, v66, 0, s[64:65]\n"
      "v_cndmask_b32 v75, 0, v67, s[64:65]\n"
      "v_cndmask_b32 v83, v67, 0, s[64:65]\n"
      "v_mov_b32 v84, 0x3f803f80\n"
      "v_mov_b32 v85, 0x3f803f80\n"
      "v_mfma_f32_16x16x16_bf16 v[196:199], v[186:187], v[68:69], 0\n"
      "s_nop 0\n"
      "v_mfma_f32_16x16x16_bf16 v[212:215], v[84:85], v[60:61], 0\n"
      "v_mfma_f32_16x16x16_bf16 v[200:203], v[188:189], v[70:71], 0\n"
      "buffer_load_dwordx4 a[248:251], v5, s[20:23], s82 offen offset:2048\n"
      "v_mfma_f32_16x16x16_bf16 v[216:219], v[84:85], v[62:63], 0\n"
      "v_mfma_f32_16x16x16_bf16 v[204:207], v[190:191], v[72:73], 0\n"
      "v_mfma_f32_16x16x16_bf16 v[220:223], v[84:85], v[64:65], 0\n"
      "v_mfma_f32_16x16x16_bf16 v[208:211], v[192:193], v[74:75], 0\n"
      "v_mfma_f32_16x16x16_bf16 v[224:227], v[84:85], v[66:67], 0\n"
      "v_mfma_f32_16x16x16_bf16 v[196:199], v[188:189], v[76:77], v[196:199]\n"
      "buffer_load_dwordx4 a[252:255], v5, s[20:23], s82 offen offset:3072\n"
      "v_mfma_f32_16x16x16_bf16 v[200:203], v[190:191], v[78:79], v[200:203]\n"
      "v_mfma_f32_16x16x16_bf16 v[204:207], v[192:193], v[80:81], v[204:207]\n"
      "v_mfma_f32_16x16x16_bf16 v[208:211], v[194:195], v[82:83], v[208:211]\n"
      "s_lshl_b32 s83, s29, 1\n"
      "s_lshr_b32 s84, s30, 1\n"
      "s_add_u32 s83, s83, s84\n"
      "s_mul_i32 s83, s83, 5120\n"
      "s_mul_i32 s84, s28, 81920\n"
      "s_add_u32 s83, s83, s84\n"
      "s_and_b32 s84, s30, 1\n"
      "s_lshl_b32 s84, s84, 9\n"
      "s_add_u32 s68, s83, s84\n"
      "s_add_u32 s69, s68, 5332992\n"
      "v_rcp_f32 v52, v212\n"
      "s_add_u32 s82, s82, 4096\n"
      "buffer_load_dwordx4 v[8:11], v5, s[20:23], s82 offen\n"
      "v_rcp_f32 v53, v216\n"
      "v_rcp_f32 v54, v220\n"
      "v_rcp_f32 v55, v224\n"
      "v_pk_mul_f32 v[196:197], v[196:197], v[52:53] op_sel:[0,0] op_sel_hi:[1,0]\n"
      "v_pk_mul_f32 v[198:199], v[198:199], v[52:53] op_sel:[0,0] op_sel_hi:[1,0]\n"
      "v_pk_mul_f32 v[200:201], v[200:201], v[52:53] op_sel:[0,1] op_sel_hi:[1,1]\n"
      "v_pk_mul_f32 v[202:203], v[202:203], v[52:53] op_sel:[0,1] op_sel_hi:[1,1]\n"
      "v_pk_mul_f32 v[204:205], v[204:205], v[54:55] op_sel:[0,0] op_sel_hi:[1,0]\n"
      "v_pk_mul_f32 v[206:207], v[206:207], v[54:55] op_sel:[0,0] op_sel_hi:[1,0]\n"
      "v_pk_mul_f32 v[208:209], v[208:209], v[54:55] op_sel:[0,1] op_sel_hi:[1,1]\n"
      "v_pk_mul_f32 v[210:211], v[210:211], v[54:55] op_sel:[0,1] op_sel_hi:[1,1]\n"
      "v_cvt_pk_bf16_f32 v88, v196, v197\n"
      "v_cvt_pk_bf16_f32 v89, v198, v199\n"
      "v_cvt_pk_bf16_f32 v90, v200, v201\n"
      "v_cvt_pk_bf16_f32 v91, v202, v203\n"
      "v_cvt_pk_bf16_f32 v92, v204, v205\n"
      "v_cvt_pk_bf16_f32 v93, v206, v207\n"
      "v_cvt_pk_bf16_f32 v94, v208, v209\n"
      "v_cvt_pk_bf16_f32 v95, v210, v211\n"
      "v_permlane16_swap_b32 v88, v90\n"
      "v_permlane16_swap_b32 v89, v91\n"
      "v_permlane16_swap_b32 v92, v94\n"
      "v_permlane16_swap_b32 v93, v95\n"
      "buffer_store_dwordx4 v[88:91], v6, s[20:23], s69 offen sc1\n"
      "buffer_store_dwordx4 v[92:95], v6, s[20:23], s69 offen offset:2048 sc1\n"
      "; X1\n"
      "buffer_load_dwordx4 v[12:15], v5, s[20:23], s82 offen offset:1024\n"
      "buffer_load_dwordx4 v[16:19], v5, s[20:23], s82 offen offset:2048\n"
      "buffer_load_dwordx4 v[20:23], v5, s[20:23], s82 offen offset:3072\n"
      "s_add_u32 s82, s82, 4096\n"
      "buffer_load_dwordx4 v[24:27], v5, s[20:23], s82 offen\n"
      "buffer_load_dwordx4 v[28:31], v5, s[20:23], s82 offen offset:1024\n"
      "buffer_load_dwordx4 v[32:35], v5, s[20:23], s82 offen offset:2048\n"
      "s_waitcnt vmcnt(6)\n"
      "s_barrier\n"
      "s_cmp_lg_u32 s30, 0\n"
      "s_cbranch_scc1 Lpub0_%=\n"
      "s_lshl_b32 s83, s28, 7\n"
      "s_lshl_b32 s84, s29, 2\n"
      "s_add_u32 s83, s83, s84\n"
      "s_add_u32 s83, s83, 5300224\n"
      "v_mov_b32 v52, 0\n"
      "v_mov_b32 v53, s31\n"
      "s_mov_b64 exec, 1\n"
      "buffer_store_dword v53, v52, s[20:23], s83 offen sc1\n"
      "s_mov_b64 exec, -1\n"
      "v_and_b32 v52, 7, v7\n"
      "v_lshlrev_b32 v52, 2, v52\n"
      "s_lshl_b32 s83, s28, 7\n"
      "s_add_u32 s83, s83, 5300224\n"
      "s_mov_b32 s86, 0\n"
      "s_nop 0\n"
      "Lpoll1_%=:\n"
      "buffer_load_dword v53, v52, s[20:23], s83 offen sc1\n"
      "s_add_u32 s86, s86, 1\n"
      "s_waitcnt vmcnt(0)\n"
      "v_cmp_ne_u32 vcc, s31, v53\n"
      "s_cmp_gt_u32 s86, 0x10000\n"
      "s_cbranch_scc1 Lpub0_%=\n"
      "s_nop 3\n"
      "s_cbranch_vccnz Lpoll1_%=\n"
      "s_nop 0\n"
      "Lpub0_%=:\n"
      "s_barrier\n"
      "s_mul_i32 s69, s28, 81920\n"
      "s_add_u32 s69, s69, 5332992\n"
      "s_mul_i32 s84, s30, 5120\n"
      "s_add_u32 s87, s69, s84\n"
      "s_lshl_b32 s84, s30, 12\n"
      "s_add_u32 m0, s84, 81920\n"
      "s_nop 0\n"
      "buffer_load_dwordx4 v5, s[20:23], s87 offen sc1 lds\n"
      "buffer_load_dwordx4 v5, s[20:23], s87 offen offset:1024 sc1 lds\n"
      "buffer_load_dwordx4 v5, s[20:23], s87 offen offset:2048 sc1 lds\n"
      "buffer_load_dwordx4 v5, s[20:23], s87 offen offset:3072 sc1 lds\n"
      "s_add_u32 m0, m0, 16384\n"
      "s_add_u32 s87, s87, 20480\n"
      "buffer_load_dwordx4 v5, s[20:23], s87 offen sc1 lds\n"
      "buffer_load_dwordx4 v5, s[20:23], s87 offen offset:1024 sc1 lds\n"
      "buffer_load_dwordx4 v5, s[20:23], s87 offen offset:2048 sc1 lds\n"
      "buffer_load_dwordx4 v5, s[20:23], s87 offen offset:3072 sc1 lds\n"
      "; P2\n"
      "v_add_u32 v52, 81920, v5\n"
      "s_lshl_b32 s83, s29, 3\n"
      "s_lshl_b32 s84, s30, 1\n"
      "s_add_u32 s83, s83, s84\n"
      "v_lshrrev_b32 v50, 1, v49\n"
      "v_or_b32 v50, s83, v50\n"
      "v_xor_b32 v50, v50, v48\n"
      "v_lshlrev_b32 v50, 4, v50\n"
      "v_and_b32 v51, 1, v49\n"
      "v_lshl_add_u32 v50, v51, 3, v50\n"
      "v_add_u32 v51, 16, v48\n"
      "v_lshl_add_u32 v53, v51, 10, v50\n"
      "ds_read_b64 v[54:55], v53\n"
      "ds_read_b64 v[56:57], v53 offset:16384\n"
      "ds_read_b64 v[58:59], v53 offset:32768\n"
      "ds_read_b64 v[60:61], v53 offset:49152\n"
      "s_waitcnt lgkmcnt(0)\n"
      "v_lshlrev_b32 v64, 16, v54\n"
      "v_and_b32 v65, 0xffff0000, v54\n"
      "v_lshlrev_b32 v66, 16, v55\n"
      "s_add_u32 m0, m0, 16384\n"
      "s_add_u32 s87, s87, 20480\n"
      "buffer_load_dwordx4 v5, s[20:23], s87 offen sc1 lds\n"
      "v_and_b32 v67, 0xffff0000, v55\n"
      "v_lshlrev_b32 v68, 16, v56\n"
      "v_and_b32 v69, 0xffff0000, v56\n"
      "v_lshlrev_b32 v70, 16, v57\n"
      "v_and_b32 v71, 0xffff0000, v57\n"
      "v_lshlrev_b32 v72, 16, v58\n"
      "v_and_b32 v73, 0xffff0000, v58\n"
      "v_lshlrev_b32 v74, 16, v59\n"
      "v_and_b32 v75, 0xffff0000, v59\n"
      "v_lshlrev_b32 v76, 16, v60\n"
      "v_and_b32 v77, 0xffff0000, v60\n"
      "v_lshlrev_b32 v78, 16, v61\n"
      "v_and_b32 v79, 0xffff0000, v61\n"
      "s_waitcnt vmcnt(5)\n"
      "s_barrier\n"
      "ds_read_b128 v[88:91], v52\n"
      "ds_read_b128 v[92:95], v52 offset:1024\n"
      "ds_read_b128 v[96:99], v52 offset:2048\n"
      "ds_read_b128 v[100:103], v52 offset:3072\n"
      "ds_read_b128 v[104:107], v52 offset:4096\n"
      "ds_read_b128 v[108:111], v52 offset:5120\n"
      "ds_read_b128 v[112:115], v52 offset:6144\n"
      "ds_read_b128 v[116:119], v52 offset:7168\n"
      "s_waitcnt lgkmcnt(4)\n"
      "v_mfma_f32_16x16x32_bf16 v[56:59], a[204:207], v[88:91], 0\n"
      "ds_read_b128 v[136:139], v52 offset:8192\n"
      "buffer_load_dwordx4 v5, s[20:23], s87 offen offset:1024 sc1 lds\n"
      "v_mfma_f32_16x16x32_bf16 v[60:63], a[204:207], v[92:95], 0\n"
      "ds_read_b128 v[140:143], v52 offset:9216\n"
      "buffer_load_dwordx4 v5, s[20:23], s87 offen offset:2048 sc1 lds\n"
      "v_mfma_f32_16x16x32_bf16 v[80:83], a[204:207], v[96:99], 0\n"
      "ds_read_b128 v[144:147], v52 offset:10240\n"
      "buffer_load_dwordx4 v5, s[20:23], s87 offen offset:3072 sc1 lds\n"
      "v_mfma_f32_16x16x32_bf16 v[84:87], a[204:207], v[100:103], 0\n"
      "ds_read_b128 v[148:151], v52 offset:11264\n"
      "s_waitcnt lgkmcnt(4)\n"
      "v_mfma_f32_16x16x32_bf16 v[56:59], a[208:211], v[104:107], v[56:59]\n"
      "ds_read_b128 v[88:91], v52 offset:12288\n"
      "s_add_u32 m0, m0, 16384\n"
      "v_mfma_f32_16x16x32_bf16 v[60:63], a[208:211], v[108:111], v[60:63]\n"
      "ds_read_b128 v[92:95], v52 offset:13312\n"
      "s_add_u32 s87, s87, 20480\n"
      "v_mfma_f32_16x16x32_bf16 v[80:83], a[208:211], v[112:115], v[80:83]\n"
      "ds_read_b128 v[96:99], v52 offset:14336\n"
      "buffer_load_dwordx4 v5, s[20:23], s87 offen sc1 lds\n"
      "v_mfma_f32_16x16x32_bf16 v[84:87], a[208:211], v[116:119], v[84:87]\n"
      "ds_read_b128 v[100:103], v52 offset:15360\n"
      "s_waitcnt vmcnt(5)\n"
      "s_barrier\n"
      "s_waitcnt lgkmcnt(4)\n"
      "v_mfma_f32_16x16x32_bf16 v[56:59], a[212:215], v[136:139], v[56:59]\n"
      "ds_read_b128 v[104:107], v52 offset:16384\n"
      "buffer_load_dwordx4 v5, s[20:23], s87 offen offset:1024 sc1 lds\n"
      "v_mfma_f32_16x16x32_bf16 v[60:63], a[212:215], v[140:143], v[60:63]\n"
      "ds_read_b128 v[108:111], v52 offset:17408\n"
      "buffer_load_dwordx4 v5, s[20:23], s87 offen offset:2048 sc1 lds\n"
      "v_mfma_f32_16x16x32_bf16 v[80:83], a[212:215], v[144:147], v[80:83]\n"
      "ds_read_b128 v[112:115], v52 offset:18432\n"
      "buffer_load_dwordx4 v5, s[20:23], s87 offen offset:3072 sc1 lds\n"
      "v_mfma_f32_16x16x32_bf16 v[84:87], a[212:215], v[148:151], v[84:87]\n"
      "ds_read_b128 v[116:119], v52 offset:19456\n"
      "s_waitcnt lgkmcnt(4)\n"
      "v_mfma_f32_16x16x32_bf16 v[56:59], a[216:219], v[88:91], v[56:59]\n"
      "ds_read_b128 v[136:139], v52 offset:20480\n"
      "buffer_load_dwordx4 v[36:39], v5, s[20:23], s82 offen offset:3072\n"
      "v_mfma_f32_16x16x32_bf16 v[60:63], a[216:219], v[92:95], v[60:63]\n"
      "ds_read_b128 v[140:143], v52 offset:21504\n"
      "s_add_u32 s82, s82, 4096\n"
      "v_mfma_f32_16x16x32_bf16 v[80:83], a[216:219], v[96:99], v[80:83]\n"
      "ds_read_b128 v[144:147], v52 offset:22528\n"
      "buffer_load_dwordx4 v[40:43], v5, s[20:23], s82 offen\n"
      "v_mfma_f32_16x16x32_bf16 v[84:87], a[216:219], v[100:103], v[84:87]\n"
      "ds_read_b128 v[148:151], v52 offset:23552\n"
      "s_waitcnt lgkmcnt(4)\n"
      "v_mfma_f32_16x16x32_bf16 v[56:59], a[220:223], v[104:107], v[56:59]\n"
      "ds_read_b128 v[88:91], v52 offset:24576\n"
      "buffer_load_dwordx4 v[44:47], v5, s[20:23], s82 offen offset:1024\n"
      "v_mfma_f32_16x16x32_bf16 v[60:63], a[220:223], v[108:111], v[60:63]\n"
      "ds_read_b128 v[92:95], v52 offset:25600\n"
      "v_mfma_f32_16x16x32_bf16 v[80:83], a[220:223], v[112:115], v[80:83]\n"
      "ds_read_b128 v[96:99], v52 offset:26624\n"
      "v_mfma_f32_16x16x32_bf16 v[84:87], a[220:223], v[116:119], v[84:87]\n"
      "ds_read_b128 v[100:103], v52 offset:27648\n"
      "s_waitcnt lgkmcnt(4)\n"
      "v_mfma_f32_16x16x32_bf16 v[56:59], a[224:227], v[136:139], v[56:59]\n"
      "ds_read_b128 v[104:107], v52 offset:28672\n"
      "buffer_load_dwordx4 a[0:3], v5, s[20:23], s82 offen offset:2048\n"
      "v_mfma_f32_16x16x32_bf16 v[60:63], a[224:227], v[140:143], v[60:63]\n"
      "ds_read_b128 v[108:111], v52 offset:29696\n"
      "v_mfma_f32_16x16x32_bf16 v[80:83], a[224:227], v[144:147], v[80:83]\n"
      "ds_read_b128 v[112:115], v52 offset:30720\n"
      "v_mfma_f32_16x16x32_bf16 v[84:87], a[224:227], v[148:151], v[84:87]\n"
      "ds_read_b128 v[116:119], v52 offset:31744\n"
      "s_waitcnt vmcnt(8)\n"
      "s_barrier\n"
      "s_waitcnt lgkmcnt(4)\n"
      "v_mfma_f32_16x16x32_bf16 v[56:59], a[228:231], v[88:91], v[56:59]\n"
      "ds_read_b128 v[136:139], v52 offset:32768\n"
      "buffer_load_dwordx4 a[4:7], v5, s[20:23], s82 offen offset:3072\n"
      "v_mfma_f32_16x16x32_bf16 v[60:63], a[228:231], v[92:95], v[60:63]\n"
      "ds_read_b128 v[140:143], v52 offset:33792\n"
      "v_mfma_f32_16x16x32_bf16 v[80:83], a[228:231], v[96:99], v[80:83]\n"
      "ds_read_b128 v[144:147], v52 offset:34816\n"
      "v_mfma_f32_16x16x32_bf16 v[84:87], a[228:231], v[100:103], v[84:87]\n"
      "ds_read_b128 v[148:151], v52 offset:35840\n"
      "s_waitcnt lgkmcnt(4)\n"
      "v_mfma_f32_16x16x32_bf16 v[56:59], a[232:235], v[104:107], v[56:59]\n"
      "ds_read_b128 v[88:91], v52 offset:36864\n"
      "s_add_u32 s82, s82, 4096\n"
      "v_mfma_f32_16x16x32_bf16 v[60:63], a[232:235], v[108:111], v[60:63]\n"
      "ds_read_b128 v[92:95], v52 offset:37888\n"
      "v_mfma_f32_16x16x32_bf16 v[80:83], a[232:235], v[112:115], v[80:83]\n"
      "buffer_load_dwordx4 a[8:11], v5, s[20:23], s82 offen\n"
      "ds_read_b128 v[96:99], v52 offset:38912\n"
      "v_mfma_f32_16x16x32_bf16 v[84:87], a[232:235], v[116:119], v[84:87]\n"
      "ds_read_b128 v[100:103], v52 offset:39936\n"
      "s_waitcnt lgkmcnt(4)\n"
      "v_mfma_f32_16x16x32_bf16 v[56:59], a[236:239], v[136:139], v[56:59]\n"
      "ds_read_b128 v[104:107], v52 offset:40960\n"
      "buffer_load_dwordx4 a[12:15], v5, s[20:23], s82 offen offset:1024\n"
      "v_mfma_f32_16x16x32_bf16 v[60:63], a[236:239], v[140:143], v[60:63]\n"
      "ds_read_b128 v[108:111], v52 offset:41984\n"
      "v_mfma_f32_16x16x32_bf16 v[80:83], a[236:239], v[144:147], v[80:83]\n"
      "ds_read_b128 v[112:115], v52 offset:43008\n"
      "v_mfma_f32_16x16x32_bf16 v[84:87], a[236:239], v[148:151], v[84:87]\n"
      "ds_read_b128 v[116:119], v52 offset:44032\n"
      "s_waitcnt lgkmcnt(4)\n"
      "v_mfma_f32_16x16x32_bf16 v[56:59], a[240:243], v[88:91], v[56:59]\n"
      "ds_read_b128 v[136:139], v52 offset:45056\n"
      "buffer_load_dwordx4 a[16:19], v5, s[20:23], s82 offen offset:2048\n"
      "v_mfma_f32_16x16x32_bf16 v[60:63], a[240:243], v[92:95], v[60:63]\n"
      "ds_read_b128 v[140:143], v52 offset:46080\n"
      "v_mfma_f32_16x16x32_bf16 v[80:83], a[240:243], v[96:99], v[80:83]\n"
      "ds_read_b128 v[144:147], v52 offset:47104\n"
      "v_mfma_f32_16x16x32_bf16 v[84:87], a[240:243], v[100:103], v[84:87]\n"
      "ds_read_b128 v[148:151], v52 offset:48128\n"
      "s_waitcnt vmcnt(8)\n"
      "s_barrier\n"
      "s_waitcnt lgkmcnt(4)\n"
      "v_mfma_f32_16x16x32_bf16 v[56:59], a[244:247], v[104:107], v[56:59]\n"
      "ds_read_b128 v[88:91], v52 offset:49152\n"
      "buffer_load_dwordx4 a[20:23], v5, s[20:23], s82 offen offset:3072\n"
      "v_mfma_f32_16x16x32_bf16 v[60:63], a[244:247], v[108:111], v[60:63]\n"
      "ds_read_b128 v[92:95], v52 offset:50176\n"
      "v_mfma_f32_16x16x32_bf16 v[80:83], a[244:247], v[112:115], v[80:83]\n"
      "ds_read_b128 v[96:99], v52 offset:51200\n"
      "v_mfma_f32_16x16x32_bf16 v[84:87], a[244:247], v[116:119], v[84:87]\n"
      "ds_read_b128 v[100:103], v52 offset:52224\n"
      "s_waitcnt lgkmcnt(4)\n"
      "v_mfma_f32_16x16x32_bf16 v[56:59], a[248:251], v[136:139], v[56:59]\n"
      "ds_read_b128 v[104:107], v52 offset:53248\n"
      "s_add_u32 s82, s82, 4096\n"
      "v_mfma_f32_16x16x32_bf16 v[60:63], a[248:251], v[140:143], v[60:63]\n"
      "ds_read_b128 v[108:111], v52 offset:54272\n"
      "v_mfma_f32_16x16x32_bf16 v[80:83], a[248:251], v[144:147], v[80:83]\n"
      "buffer_load_dwordx4 a[24:27], v5, s[20:23], s82 offen\n"
      "ds_read_b128 v[112:115], v52 offset:55296\n"
      "v_mfma_f32_16x16x32_bf16 v[84:87], a[248:251], v[148:151], v[84:87]\n"
      "ds_read_b128 v[116:119], v52 offset:56320\n"
      "s_waitcnt lgkmcnt(4)\n"
      "v_mfma_f32_16x16x32_bf16 v[56:59], a[252:255], v[88:91], v[56:59]\n"
      "ds_read_b128 v[136:139], v52 offset:57344\n"
      "buffer_load_dwordx4 a[28:31], v5, s[20:23], s82 offen offset:1024\n"
      "v_mfma_f32_16x16x32_bf16 v[60:63], a[252:255], v[92:95], v[60:63]\n"
      "ds_read_b128 v[140:143], v52 offset:58368\n"
      "v_mfma_f32_16x16x32_bf16 v[80:83], a[252:255], v[96:99], v[80:83]\n"
      "ds_read_b128 v[144:147], v52 offset:59392\n"
      "v_mfma_f32_16x16x32_bf16 v[84:87], a[252:255], v[100:103], v[84:87]\n"
      "ds_read_b128 v[148:151], v52 offset:60416\n"
      "s_waitcnt lgkmcnt(4)\n"
      "v_mfma_f32_16x16x32_bf16 v[56:59], v[8:11], v[104:107], v[56:59]\n"
      "ds_read_b128 v[88:91], v52 offset:61440\n"
      "buffer_load_dwordx4 a[32:35], v5, s[20:23], s82 offen offset:2048\n"
      "v_mfma_f32_16x16x32_bf16 v[60:63], v[8:11], v[108:111], v[60:63]\n"
      "ds_read_b128 v[92:95], v52 offset:62464\n"
      "v_mfma_f32_16x16x32_bf16 v[80:83], v[8:11], v[112:115], v[80:83]\n"
      "ds_read_b128 v[96:99], v52 offset:63488\n"
      "v_mfma_f32_16x16x32_bf16 v[84:87], v[8:11], v[116:119], v[84:87]\n"
      "ds_read_b128 v[100:103], v52 offset:64512\n"
      "s_waitcnt lgkmcnt(4)\n"
      "v_mfma_f32_16x16x32_bf16 v[56:59], v[12:15], v[136:139], v[56:59]\n"
      "buffer_load_dwordx4 a[36:39], v5, s[20:23], s82 offen offset:3072\n"
      "v_mfma_f32_16x16x32_bf16 v[60:63], v[12:15], v[140:143], v[60:63]\n"
      "v_mfma_f32_16x16x32_bf16 v[80:83], v[12:15], v[144:147], v[80:83]\n"
      "v_mfma_f32_16x16x32_bf16 v[84:87], v[12:15], v[148:151], v[84:87]\n"
      "s_waitcnt lgkmcnt(0)\n"
      "v_mfma_f32_16x16x32_bf16 v[56:59], v[16:19], v[88:91], v[56:59]\n"
      "s_add_u32 s82, s82, 4096\n"
      "v_mfma_f32_16x16x32_bf16 v[60:63], v[16:19], v[92:95], v[60:63]\n"
      "v_mfma_f32_16x16x32_bf16 v[80:83], v[16:19], v[96:99], v[80:83]\n"
      "buffer_load_dwordx4 a[40:43], v5, s[20:23], s82 offen\n"
      "v_mfma_f32_16x16x32_bf16 v[84:87], v[16:19], v[100:103], v[84:87]\n"
      "; P2 epilogue\n"
      "s_nop 2\n"
      "v_pk_fma_f32 v[56:57], v[120:121], v[56:57], v[64:65] op_sel:[0,0,0] op_sel_hi:[1,1,1]\n"
      "v_pk_fma_f32 v[58:59], v[122:123], v[58:59], v[66:67] op_sel:[0,0,0] op_sel_hi:[1,1,1]\n"
      "v_pk_fma_f32 v[60:61], v[120:121], v[60:61], v[68:69] op_sel:[0,0,0] op_sel_hi:[1,1,1]\n"
      "v_pk_fma_f32 v[62:63], v[122:123], v[62:63], v[70:71] op_sel:[0,0,0] op_sel_hi:[1,1,1]\n"
      "v_pk_fma_f32 v[80:81], v[120:121], v[80:81], v[72:73] op_sel:[0,0,0] op_sel_hi:[1,1,1]\n"
      "v_pk_fma_f32 v[82:83], v[122:123], v[82:83], v[74:75] op_sel:[0,0,0] op_sel_hi:[1,1,1]\n"
      "v_pk_fma_f32 v[84:85], v[120:121], v[84:85], v[76:77] op_sel:[0,0,0] op_sel_hi:[1,1,1]\n"
      "v_pk_fma_f32 v[86:87], v[122:123], v[86:87], v[78:79] op_sel:[0,0,0] op_sel_hi:[1,1,1]\n"
      "s_add_u32 s83, s68, 7954432\n"
      "v_cvt_pk_bf16_f32 v92, v56, v57\n"
      "v_cvt_pk_bf16_f32 v93, v58, v59\n"
      "v_cvt_pk_bf16_f32 v94, v60, v61\n"
      "v_cvt_pk_bf16_f32 v95, v62, v63\n"
      "v_cvt_pk_bf16_f32 v96, v80, v81\n"
      "v_cvt_pk_bf16_f32 v97, v82, v83\n"
      "v_cvt_pk_bf16_f32 v98, v84, v85\n"
      "v_cvt_pk_bf16_f32 v99, v86, v87\n"
      "v_mov_b32 v52, v92\n"
      "v_mov_b32 v53, v93\n"
      "v_mov_b32 v54, v94\n"
      "v_mov_b32 v55, v95\n"
      "v_mov_b32 v88, v96\n"
      "v_mov_b32 v89, v97\n"
      "v_mov_b32 v90, v98\n"
      "v_mov_b32 v91, v99\n"
      "v_permlane16_swap_b32 v92, v94\n"
      "v_permlane16_swap_b32 v93, v95\n"
      "v_permlane16_swap_b32 v96, v98\n"
      "v_permlane16_swap_b32 v97, v99\n"
      "buffer_store_dwordx4 v[92:95], v6, s[20:23], s83 offen sc1\n"
      "buffer_store_dwordx4 v[96:99], v6, s[20:23], s83 offen offset:2048 sc1\n"
      "v_lshlrev_b32 v56, 16, v52\n"
      "v_and_b32 v57, 0xffff0000, v52\n"
      "v_lshlrev_b32 v58, 16, v53\n"
      "v_and_b32 v59, 0xffff0000, v53\n"
      "v_lshlrev_b32 v60, 16, v54\n"
      "v_and_b32 v61, 0xffff0000, v54\n"
      "v_lshlrev_b32 v62, 16, v55\n"
      "v_and_b32 v63, 0xffff0000, v55\n"
      "v_lshlrev_b32 v80, 16, v88\n"
      "v_and_b32 v81, 0xffff0000, v88\n"
      "v_lshlrev_b32 v82, 16, v89\n"
      "v_and_b32 v83, 0xffff0000, v89\n"
      "v_lshlrev_b32 v84, 16, v90\n"
      "v_and_b32 v85, 0xffff0000, v90\n"
      "v_lshlrev_b32 v86, 16, v91\n"
      "v_and_b32 v87, 0xffff0000, v91\n"
      "v_pk_add_f32 v[64:65], v[56:57], v[58:59] op_sel:[0,0] op_sel_hi:[1,1]\n"
      "v_pk_add_f32 v[68:69], v[60:61], v[62:63] op_sel:[0,0] op_sel_hi:[1,1]\n"
      "v_pk_add_f32 v[72:73], v[80:81], v[82:83] op_sel:[0,0] op_sel_hi:[1,1]\n"
      "v_pk_add_f32 v[76:77], v[84:85], v[86:87] op_sel:[0,0] op_sel_hi:[1,1]\n"
      "v_pk_mul_f32 v[66:67], v[56:57], v[56:57] op_sel:[0,0] op_sel_hi:[1,1]\n"
      "v_pk_mul_f32 v[70:71], v[60:61], v[60:61] op_sel:[0,0] op_sel_hi:[1,1]\n"
      "v_pk_mul_f32 v[74:75], v[80:81], v[80:81] op_sel:[0,0] op_sel_hi:[1,1]\n"
      "v_pk_mul_f32 v[78:79], v[84:85], v[84:85] op_sel:[0,0] op_sel_hi:[1,1]\n"
      "v_add_f32 v100, v64, v65\n"
      "v_add_f32 v101, v68, v69\n"
      "v_add_f32 v102, v72, v73\n"
      "v_add_f32 v103, v76, v77\n"
      "v_pk_fma_f32 v[66:67], v[58:59], v[58:59], v[66:67] op_sel:[0,0,0] op_sel_hi:[1,1,1]\n"
      "v_pk_fma_f32 v[70:71], v[62:63], v[62:63], v[70:71] op_sel:[0,0,0] op_sel_hi:[1,1,1]\n"
      "v_pk_fma_f32 v[74:75], v[82:83], v[82:83], v[74:75] op_sel:[0,0,0] op_sel_hi:[1,1,1]\n"
      "v_pk_fma_f32 v[78:79], v[86:87], v[86:87], v[78:79] op_sel:[0,0,0] op_sel_hi:[1,1,1]\n"
      "v_add_f32 v104, v66, v67\n"
      "v_add_f32 v105, v70, v71\n"
      "v_add_f32 v106, v74, v75\n"
      "v_add_f32 v107, v78, v79\n"
      "v_permlane32_swap_b32 v100, v102\n"
      "v_permlane32_swap_b32 v101, v103\n"
      "v_permlane32_swap_b32 v104, v106\n"
      "v_permlane32_swap_b32 v105, v107\n"
      "v_add_f32 v100, v100, v102\n"
      "v_add_f32 v101, v101, v103\n"
      "v_add_f32 v104, v104, v106\n"
      "v_add_f32 v105, v105, v107\n"
      "v_permlane16_swap_b32 v100, v101\n"
      "s_nop 0\n"
      "v_permlane16_swap_b32 v104, v105\n"
      "v_add_f32 v108, v100, v101\n"
      "v_add_f32 v109, v104, v105\n"
      "s_lshl_b32 s83, s28, 14\n"
      "s_lshl_b32 s84, s29, 2\n"
      "s_add_u32 s84, s84, s30\n"
      "s_lshl_b32 s84, s84, 9\n"
      "s_add_u32 s83, s83, s84\n"
      "s_add_u32 s83, s83, 10575872\n"
      "v_lshlrev_b32 v50, 3, v7\n"
      "buffer_store_dwordx2 v[108:109], v50, s[20:23], s83 offen sc1\n"
      "; X2\n"
      "buffer_load_dwordx4 a[44:47], v5, s[20:23], s82 offen offset:1024\n"
      "buffer_load_dwordx4 a[48:51], v5, s[20:23], s82 offen offset:2048\n"
      "buffer_load_dwordx4 a[52:55], v5, s[20:23], s82 offen offset:3072\n"
      "s_add_u32 s82, s82, 4096\n"
      "buffer_load_dwordx4 a[56:59], v5, s[20:23], s82 offen\n"
      "buffer_load_dwordx4 a[60:63], v5, s[20:23], s82 offen offset:1024\n"
      "buffer_load_dwordx4 a[64:67], v5, s[20:23], s82 offen offset:2048\n"
      "s_waitcnt vmcnt(6)\n"
      "s_barrier\n"
      "s_cmp_lg_u32 s30, 0\n"
      "s_cbranch_scc1 Lpub1_%=\n"
      "s_lshl_b32 s83, s28, 7\n"
      "s_lshl_b32 s84, s29, 2\n"
      "s_add_u32 s83, s83, s84\n"
      "s_add_u32 s83, s83, 5304320\n"
      "v_mov_b32 v56, 0\n"
      "v_mov_b32 v57, s31\n"
      "s_mov_b64 exec, 1\n"
      "buffer_store_dword v57, v56, s[20:23], s83 offen sc1\n"
      "s_mov_b64 exec, -1\n"
      "v_and_b32 v56, 7, v7\n"
      "v_lshlrev_b32 v56, 2, v56\n"
      "s_lshl_b32 s83, s28, 7\n"
      "s_add_u32 s83, s83, 5304320\n"
      "s_mov_b32 s86, 0\n"
      "s_nop 0\n"
      "Lpoll2_%=:\n"
      "buffer_load_dword v57, v56, s[20:23], s83 offen sc1\n"
      "s_add_u32 s86, s86, 1\n"
      "s_waitcnt vmcnt(0)\n"
      "v_cmp_ne_u32 vcc, s31, v57\n"
      "s_cmp_gt_u32 s86, 0x10000\n"
      "s_cbranch_scc1 Lpub1_%=\n"
      "s_nop 3\n"
      "s_cbranch_vccnz Lpoll2_%=\n"
      "s_nop 0\n"
      "Lpub1_%=:\n"
      "s_barrier\n"
      "s_mul_i32 s69, s28, 81920\n"
      "s_add_u32 s69, s69, 7954432\n"
      "s_mul_i32 s84, s30, 5120\n"
      "s_add_u32 s87, s69, s84\n"
      "s_lshl_b32 s84, s30, 12\n"
      "s_add_u32 m0, s84, 0\n"
      "s_nop 0\n"
      "buffer_load_dwordx4 v5, s[20:23], s87 offen sc1 lds\n"
      "buffer_load_dwordx4 v5, s[20:23], s87 offen offset:1024 sc1 lds\n"
      "buffer_load_dwordx4 v5, s[20:23], s87 offen offset:2048 sc1 lds\n"
      "buffer_load_dwordx4 v5, s[20:23], s87 offen offset:3072 sc1 lds\n"
      "s_add_u32 m0, m0, 16384\n"
      "s_add_u32 s87, s87, 20480\n"
      "buffer_load_dwordx4 v5, s[20:23], s87 offen sc1 lds\n"
      "buffer_load_dwordx4 v5, s[20:23], s87 offen offset:1024 sc1 lds\n"
      "buffer_load_dwordx4 v5, s[20:23], s87 offen offset:2048 sc1 lds\n"
      "buffer_load_dwordx4 v5, s[20:23], s87 offen offset:3072 sc1 lds\n"
      "v_lshlrev_b32 v50, 12, v49\n"
      "v_lshl_add_u32 v50, v48, 3, v50\n"
      "s_lshl_b32 s83, s28, 14\n"
      "s_lshl_b32 s84, s30, 7\n"
      "s_add_u32 s73, s83, s84\n"
      "s_add_u32 s73, s73, 10575872\n"
      "v_mov_b32 v72, v50\n"
      "; P3\n"
      "v_mfma_f32_16x16x32_bf16 v[172:175], v[20:23], v[128:131], 0\n"
      "v_mfma_f32_16x16x32_bf16 v[176:179], v[20:23], v[132:135], 0\n"
      "v_mfma_f32_16x16x32_bf16 v[180:183], v[24:27], v[128:131], 0\n"
      "v_mfma_f32_16x16x32_bf16 v[184:187], v[24:27], v[132:135], 0\n"
      "v_mfma_f32_16x16x32_bf16 v[188:191], v[28:31], v[128:131], 0\n"
      "v_mfma_f32_16x16x32_bf16 v[192:195], v[28:31], v[132:135], 0\n"
      "s_waitcnt vmcnt(4)\n"
      "s_barrier\n"
      "ds_read_b128 v[140:143], v5\n"
      "ds_read_b128 v[144:147], v5 offset:1024\n"
      "ds_read_b128 v[148:151], v5 offset:2048\n"
      "ds_read_b128 v[152:155], v5 offset:3072\n"
      "s_waitcnt lgkmcnt(3)\n"
      "v_mfma_f32_16x16x32_bf16 v[76:79], v[32:35], v[140:143], 0\n"
      "ds_read_b128 v[156:159], v5 offset:4096\n"
      "v_mfma_f32_16x16x32_bf16 v[96:99], v[36:39], v[140:143], 0\n"
      "s_add_u32 m0, m0, 16384\n"
      "v_mfma_f32_16x16x32_bf16 v[112:115], v[40:43], v[140:143], 0\n"
      "s_waitcnt lgkmcnt(3)\n"
      "v_mfma_f32_16x16x32_bf16 v[80:83], v[32:35], v[144:147], 0\n"
      "ds_read_b128 v[160:163], v5 offset:5120\n"
      "v_mfma_f32_16x16x32_bf16 v[100:103], v[36:39], v[144:147], 0\n"
      "s_add_u32 s87, s87, 20480\n"
      "v_mfma_f32_16x16x32_bf16 v[116:119], v[40:43], v[144:147], 0\n"
      "s_waitcnt lgkmcnt(3)\n"
      "v_mfma_f32_16x16x32_bf16 v[84:87], v[32:35], v[148:151], 0\n"
      "ds_read_b128 v[164:167], v5 offset:6144\n"
      "v_mfma_f32_16x16x32_bf16 v[104:107], v[36:39], v[148:151], 0\n"
      "buffer_load_dwordx4 v5, s[20:23], s87 offen sc1 lds\n"
      "v_mfma_f32_16x16x32_bf16 v[120:123], v[40:43], v[148:151], 0\n"
      "s_waitcnt lgkmcnt(3)\n"
      "v_mfma_f32_16x16x32_bf16 v[92:95], v[32:35], v[152:155], 0\n"
      "ds_read_b128 v[168:171], v5 offset:7168\n"
      "v_mfma_f32_16x16x32_bf16 v[108:111], v[36:39], v[152:155], 0\n"
      "buffer_load_dwordx4 v5, s[20:23], s87 offen offset:1024 sc1 lds\n"
      "v_mfma_f32_16x16x32_bf16 v[136:139], v[40:43], v[152:155], 0\n"
      "s_waitcnt lgkmcnt(3)\n"
      "v_mfma_f32_16x16x32_bf16 v[76:79], v[44:47], v[156:159], v[76:79]\n"
      "ds_read_b128 v[140:143], v5 offset:8192\n"
      "buffer_load_dwordx4 v5, s[20:23], s87 offen offset:2048 sc1 lds\n"
      "v_mfma_f32_16x16x32_bf16 v[96:99], a[0:3], v[156:159], v[96:99]\n"
      "buffer_load_dwordx2 v[56:57], v72, s[20:23], s73 offen sc1\n"
      "v_mfma_f32_16x16x32_bf16 v[112:115], a[4:7], v[156:159], v[112:115]\n"
      "buffer_load_dwordx2 v[58:59], v72, s[20:23], s73 offen offset:512 sc1\n"
      "s_waitcnt lgkmcnt(3)\n"
      "v_mfma_f32_16x16x32_bf16 v[80:83], v[44:47], v[160:163], v[80:83]\n"
      "ds_read_b128 v[144:147], v5 offset:9216\n"
      "v_mfma_f32_16x16x32_bf16 v[100:103], a[0:3], v[160:163], v[100:103]\n"
      "buffer_load_dwordx4 v5, s[20:23], s87 offen offset:3072 sc1 lds\n"
      "v_mfma_f32_16x16x32_bf16 v[116:119], a[4:7], v[160:163], v[116:119]\n"
      "buffer_load_dwordx2 v[60:61], v72, s[20:23], s73 offen offset:1024 sc1\n"
      "s_waitcnt lgkmcnt(3)\n"
      "v_mfma_f32_16x16x32_bf16 v[84:87], v[44:47], v[164:167], v[84:87]\n"
      "buffer_load_dwordx2 v[62:63], v72, s[20:23], s73 offen offset:1536 sc1\n"
      "v_mfma_f32_16x16x32_bf16 v[104:107], a[0:3], v[164:167], v[104:107]\n"
      "ds_read_b128 v[148:151], v5 offset:10240\n"
      "v_mfma_f32_16x16x32_bf16 v[120:123], a[4:7], v[164:167], v[120:123]\n"
      "s_add_u32 m0, m0, 16384\n"
      "s_waitcnt lgkmcnt(3)\n"
      "v_mfma_f32_16x16x32_bf16 v[92:95], v[44:47], v[168:171], v[92:95]\n"
      "ds_read_b128 v[152:155], v5 offset:11264\n"
      "v_mfma_f32_16x16x32_bf16 v[108:111], a[0:3], v[168:171], v[108:111]\n"
      "s_add_u32 s87, s87, 20480\n"
      "v_mfma_f32_16x16x32_bf16 v[136:139], a[4:7], v[168:171], v[136:139]\n"
      "buffer_load_dwordx4 v5, s[20:23], s87 offen sc1 lds\n"
      "s_waitcnt lgkmcnt(3)\n"
      "v_mfma_f32_16x16x32_bf16 v[76:79], a[8:11], v[140:143], v[76:79]\n"
      "ds_read_b128 v[156:159], v5 offset:12288\n"
      "v_mfma_f32_16x16x32_bf16 v[96:99], a[12:15], v[140:143], v[96:99]\n"
      "buffer_load_dwordx4 v5, s[20:23], s87 offen offset:1024 sc1 lds\n"
      "v_mfma_f32_16x16x32_bf16 v[112:115], a[16:19], v[140:143], v[112:115]\n"
      "buffer_load_dwordx2 v[64:65], v72, s[20:23], s73 offen offset:2048 sc1\n"
      "s_waitcnt lgkmcnt(3)\n"
      "v_mfma_f32_16x16x32_bf16 v[80:83], a[8:11], v[144:147], v[80:83]\n"
      "buffer_load_dwordx2 v[66:67], v72, s[20:23], s73 offen offset:2560 sc1\n"
      "v_mfma_f32_16x16x32_bf16 v[100:103], a[12:15], v[144:147], v[100:103]\n"
      "ds_read_b128 v[160:163], v5 offset:13312\n"
      "v_mfma_f32_16x16x32_bf16 v[116:119], a[16:19], v[144:147], v[116:119]\n"
      "buffer_load_dwordx4 v5, s[20:23], s87 offen offset:2048 sc1 lds\n"
      "s_waitcnt lgkmcnt(3)\n"
      "v_mfma_f32_16x16x32_bf16 v[84:87], a[8:11], v[148:151], v[84:87]\n"
      "buffer_load_dwordx2 v[68:69], v72, s[20:23], s73 offen offset:3072 sc1\n"
      "v_mfma_f32_16x16x32_bf16 v[104:107], a[12:15], v[148:151], v[104:107]\n"
      "buffer_load_dwordx2 v[70:71], v72, s[20:23], s73 offen offset:3584 sc1\n"
      "v_mfma_f32_16x16x32_bf16 v[120:123], a[16:19], v[148:151], v[120:123]\n"
      "ds_read_b128 v[164:167], v5 offset:14336\n"
      "s_waitcnt lgkmcnt(3)\n"
      "v_mfma_f32_16x16x32_bf16 v[92:95], a[8:11], v[152:155], v[92:95]\n"
      "buffer_load_dwordx4 v5, s[20:23], s87 offen offset:3072 sc1 lds\n"
      "v_mfma_f32_16x16x32_bf16 v[108:111], a[12:15], v[152:155], v[108:111]\n"
      "ds_read_b128 v[168:171], v5 offset:15360\n"
      "v_mfma_f32_16x16x32_bf16 v[136:139], a[16:19], v[152:155], v[136:139]\n"
      "buffer_load_dwordx4 a[68:71], v5, s[20:23], s82 offen offset:3072\n"
      "s_waitcnt vmcnt(17)\n"
      "s_barrier\n"
      "s_waitcnt lgkmcnt(3)\n"
      "v_mfma_f32_16x16x32_bf16 v[76:79], a[20:23], v[156:159], v[76:79]\n"
      "ds_read_b128 v[140:143], v5 offset:16384\n"
      "v_mfma_f32_16x16x32_bf16 v[96:99], a[24:27], v[156:159], v[96:99]\n"
      "s_add_u32 s82, s82, 4096\n"
      "v_mfma_f32_16x16x32_bf16 v[112:115], a[28:31], v[156:159], v[112:115]\n"
      "ds_read_b128 v[144:147], v5 offset:17408\n"
      "s_waitcnt lgkmcnt(4)\n"
      "v_mfma_f32_16x16x32_bf16 v[80:83], a[20:23], v[160:163], v[80:83]\n"
      "v_mfma_f32_16x16x32_bf16 v[100:103], a[24:27], v[160:163], v[100:103]\n"
      "buffer_load_dwordx4 a[72:75], v5, s[20:23], s82 offen\n"
      "v_mfma_f32_16x16x32_bf16 v[116:119], a[28:31], v[160:163], v[116:119]\n"
      "ds_read_b128 v[148:151], v5 offset:18432\n"
      "s_waitcnt lgkmcnt(4)\n"
      "v_mfma_f32_16x16x32_bf16 v[84:87], a[20:23], v[164:167], v[84:87]\n"
      "buffer_load_dwordx4 a[76:79], v5, s[20:23], s82 offen offset:1024\n"
      "v_mfma_f32_16x16x32_bf16 v[104:107], a[24:27], v[164:167], v[104:107]\n"
      "v_mfma_f32_16x16x32_bf16 v[120:123], a[28:31], v[164:167], v[120:123]\n"
      "ds_read_b128 v[152:155], v5 offset:19456\n"
      "s_waitcnt lgkmcnt(4)\n"
      "v_mfma_f32_16x16x32_bf16 v[92:95], a[20:23], v[168:171], v[92:95]\n"
      "buffer_load_dwordx4 a[80:83], v5, s[20:23], s82 offen offset:2048\n"
      "v_mfma_f32_16x16x32_bf16 v[108:111], a[24:27], v[168:171], v[108:111]\n"
      "buffer_load_dwordx4 a[84:87], v5, s[20:23], s82 offen offset:3072\n"
      "v_mfma_f32_16x16x32_bf16 v[136:139], a[28:31], v[168:171], v[136:139]\n"
      "s_waitcnt lgkmcnt(3)\n"
      "v_mfma_f32_16x16x32_bf16 v[76:79], a[32:35], v[140:143], v[76:79]\n"
      "ds_read_b128 v[156:159], v5 offset:20480\n"
      "v_mfma_f32_16x16x32_bf16 v[96:99], a[36:39], v[140:143], v[96:99]\n"
      "s_add_u32 s82, s82, 4096\n"
      "v_mfma_f32_16x16x32_bf16 v[112:115], a[40:43], v[140:143], v[112:115]\n"
      "s_waitcnt lgkmcnt(3)\n"
      "v_mfma_f32_16x16x32_bf16 v[80:83], a[32:35], v[144:147], v[80:83]\n"
      "ds_read_b128 v[160:163], v5 offset:21504\n"
      "v_mfma_f32_16x16x32_bf16 v[100:103], a[36:39], v[144:147], v[100:103]\n"
      "buffer_load_dwordx4 a[88:91], v5, s[20:23], s82 offen\n"
      "v_mfma_f32_16x16x32_bf16 v[116:119], a[40:43], v[144:147], v[116:119]\n"
      "s_waitcnt lgkmcnt(3)\n"
      "v_mfma_f32_16x16x32_bf16 v[84:87], a[32:35], v[148:151], v[84:87]\n"
      "ds_read_b128 v[164:167], v5 offset:22528\n"
      "v_mfma_f32_16x16x32_bf16 v[104:107], a[36:39], v[148:151], v[104:107]\n"
      "buffer_load_dwordx4 a[92:95], v5, s[20:23], s82 offen offset:1024\n"
      "v_mfma_f32_16x16x32_bf16 v[120:123], a[40:43], v[148:151], v[120:123]\n"
      "s_waitcnt lgkmcnt(3)\n"
      "v_mfma_f32_16x16x32_bf16 v[92:95], a[32:35], v[152:155], v[92:95]\n"
      "ds_read_b128 v[168:171], v5 offset:23552\n"
      "v_mfma_f32_16x16x32_bf16 v[108:111], a[36:39], v[152:155], v[108:111]\n"
      "buffer_load_dwordx4 a[96:99], v5, s[20:23], s82 offen offset:2048\n"
      "v_mfma_f32_16x16x32_bf16 v[136:139], a[40:43], v[152:155], v[136:139]\n"
      "s_waitcnt lgkmcnt(3)\n"
      "v_mfma_f32_16x16x32_bf16 v[76:79], a[44:47], v[156:159], v[76:79]\n"
      "ds_read_b128 v[140:143], v5 offset:24576\n"
      "v_mfma_f32_16x16x32_bf16 v[96:99], a[48:51], v[156:159], v[96:99]\n"
      "buffer_load_dwordx4 a[100:103], v5, s[20:23], s82 offen offset:3072\n"
      "v_mfma_f32_16x16x32_bf16 v[112:115], a[52:55], v[156:159], v[112:115]\n"
      "ds_read_b128 v[144:147], v5 offset:25600\n"
      "s_waitcnt lgkmcnt(4)\n"
      "v_mfma_f32_16x16x32_bf16 v[80:83], a[44:47], v[160:163], v[80:83]\n"
      "v_mfma_f32_16x16x32_bf16 v[100:103], a[48:51], v[160:163], v[100:103]\n"
      "s_add_u32 s82, s82, 4096\n"
      "v_mfma_f32_16x16x32_bf16 v[116:119], a[52:55], v[160:163], v[116:119]\n"
      "ds_read_b128 v[148:151], v5 offset:26624\n"
      "s_waitcnt lgkmcnt(4)\n"
      "v_mfma_f32_16x16x32_bf16 v[84:87], a[44:47], v[164:167], v[84:87]\n"
      "buffer_load_dwordx4 a[104:107], v5, s[20:23], s82 offen\n"
      "v_mfma_f32_16x16x32_bf16 v[104:107], a[48:51], v[164:167], v[104:107]\n"
      "v_mfma_f32_16x16x32_bf16 v[120:123], a[52:55], v[164:167], v[120:123]\n"
      "ds_read_b128 v[152:155], v5 offset:27648\n"
      "s_waitcnt lgkmcnt(4)\n"
      "v_mfma_f32_16x16x32_bf16 v[92:95], a[44:47], v[168:171], v[92:95]\n"
      "buffer_load_dwordx4 a[108:111], v5, s[20:23], s82 offen offset:1024\n"
      "v_mfma_f32_16x16x32_bf16 v[108:111], a[48:51], v[168:171], v[108:111]\n"
      "buffer_load_dwordx4 a[112:115], v5, s[20:23], s82 offen offset:2048\n"
      "v_mfma_f32_16x16x32_bf16 v[136:139], a[52:55], v[168:171], v[136:139]\n"
      "s_waitcnt vmcnt(13)\n"
      "v_pk_add_f32 v[56:57], v[56:57], v[58:59] op_sel:[0,0] op_sel_hi:[1,1]\n"
      "v_pk_add_f32 v[60:61], v[60:61], v[62:63] op_sel:[0,0] op_sel_hi:[1,1]\n"
      "v_pk_add_f32 v[64:65], v[64:65], v[66:67] op_sel:[0,0] op_sel_hi:[1,1]\n"
      "v_pk_add_f32 v[68:69], v[68:69], v[70:71] op_sel:[0,0] op_sel_hi:[1,1]\n"
      "v_pk_add_f32 v[56:57], v[56:57], v[60:61] op_sel:[0,0] op_sel_hi:[1,1]\n"
      "v_pk_add_f32 v[64:65], v[64:65], v[68:69] op_sel:[0,0] op_sel_hi:[1,1]\n"
      "v_pk_add_f32 v[56:57], v[56:57], v[64:65] op_sel:[0,0] op_sel_hi:[1,1]\n"
      "v_mov_b32 v58, v56\n"
      "v_mov_b32 v59, v57\n"
      "s_nop 0\n"
      "v_permlane16_swap_b32 v56, v58\n"
      "v_permlane16_swap_b32 v57, v59\n"
      "v_pk_add_f32 v[56:57], v[56:57], v[58:59] op_sel:[0,0] op_sel_hi:[1,1]\n"
      "v_mov_b32 v58, v56\n"
      "v_mov_b32 v59, v57\n"
      "s_nop 0\n"
      "v_permlane32_swap_b32 v56, v58\n"
      "v_permlane32_swap_b32 v57, v59\n"
      "v_pk_add_f32 v[56:57], v[56:57], v[58:59] op_sel:[0,0] op_sel_hi:[1,1]\n"
      "v_mul_f32 v56, 0x3b000000, v56\n"
      "v_mul_f32 v57, 0x3b000000, v57\n"
      "v_fma_f32 v57, -v56, v56, v57\n"
      "v_max_f32 v57, 0, v57\n"
      "v_add_f32 v57, 0x3727c5ac, v57\n"
      "v_rsq_f32 v57, v57\n"
      "v_lshlrev_b32 v60, 3, v48\n"
      "s_lshl_b32 s83, s30, 7\n"
      "s_add_u32 s83, s83, 160768\n"
      "v_add_u32 v60, s83, v60\n"
      "ds_write_b64 v60, v[56:57]\n"
      "s_waitcnt lgkmcnt(0)\n"
      "v_mfma_f32_16x16x32_bf16 v[76:79], a[56:59], v[140:143], v[76:79]\n"
      "ds_read_b128 v[156:159], v5 offset:28672\n"
      "v_mfma_f32_16x16x32_bf16 v[96:99], a[60:63], v[140:143], v[96:99]\n"
      "buffer_load_dwordx4 a[116:119], v5, s[20:23], s82 offen offset:3072\n"
      "v_mfma_f32_16x16x32_bf16 v[112:115], a[64:67], v[140:143], v[112:115]\n"
      "v_mfma_f32_16x16x32_bf16 v[80:83], a[56:59], v[144:147], v[80:83]\n"
      "ds_read_b128 v[160:163], v5 offset:29696\n"
      "v_mfma_f32_16x16x32_bf16 v[100:103], a[60:63], v[144:147], v[100:103]\n"
      "s_add_u32 s82, s82, 4096\n"
      "v_mfma_f32_16x16x32_bf16 v[116:119], a[64:67], v[144:147], v[116:119]\n"
      "v_mfma_f32_16x16x32_bf16 v[84:87], a[56:59], v[148:151], v[84:87]\n"
      "ds_read_b128 v[164:167], v5 offset:30720\n"
      "v_mfma_f32_16x16x32_bf16 v[104:107], a[60:63], v[148:151], v[104:107]\n"
      "buffer_load_dwordx4 a[120:123], v5, s[20:23], s82 offen\n"
      "v_mfma_f32_16x16x32_bf16 v[120:123], a[64:67], v[148:151], v[120:123]\n"
      "v_mfma_f32_16x16x32_bf16 v[92:95], a[56:59], v[152:155], v[92:95]\n"
      "ds_read_b128 v[168:171], v5 offset:31744\n"
      "v_mfma_f32_16x16x32_bf16 v[108:111], a[60:63], v[152:155], v[108:111]\n"
      "buffer_load_dwordx4 a[124:127], v5, s[20:23], s82 offen offset:1024\n"
      "v_mfma_f32_16x16x32_bf16 v[136:139], a[64:67], v[152:155], v[136:139]\n"
      "s_waitcnt vmcnt(14)\n"
      "s_waitcnt vmcnt(13)\n"
      "s_waitcnt vmcnt(12)\n"
      "s_barrier\n"
      "s_waitcnt lgkmcnt(3)\n"
      "v_mfma_f32_16x16x32_bf16 v[76:79], a[68:71], v[156:159], v[76:79]\n"
      "ds_read_b128 v[140:143], v5 offset:32768\n"
      "v_mfma_f32_16x16x32_bf16 v[96:99], a[72:75], v[156:159], v[96:99]\n"
      "buffer_load_dwordx4 a[128:131], v5, s[20:23], s82 offen offset:2048\n"
      "v_mfma_f32_16x16x32_bf16 v[112:115], a[76:79], v[156:159], v[112:115]\n"
      "ds_read_b128 v[144:147], v5 offset:33792\n"
      "s_waitcnt lgkmcnt(4)\n"
      "v_mfma_f32_16x16x32_bf16 v[80:83], a[68:71], v[160:163], v[80:83]\n"
      "v_mfma_f32_16x16x32_bf16 v[100:103], a[72:75], v[160:163], v[100:103]\n"
      "buffer_load_dwordx4 a[132:135], v5, s[20:23], s82 offen offset:3072\n"
      "v_mfma_f32_16x16x32_bf16 v[116:119], a[76:79], v[160:163], v[116:119]\n"
      "ds_read_b128 v[148:151], v5 offset:34816\n"
      "s_waitcnt lgkmcnt(4)\n"
      "v_mfma_f32_16x16x32_bf16 v[84:87], a[68:71], v[164:167], v[84:87]\n"
      "s_add_u32 s82, s82, 4096\n"
      "v_mfma_f32_16x16x32_bf16 v[104:107], a[72:75], v[164:167], v[104:107]\n"
      "v_mfma_f32_16x16x32_bf16 v[120:123], a[76:79], v[164:167], v[120:123]\n"
      "ds_read_b128 v[152:155], v5 offset:35840\n"
      "s_waitcnt lgkmcnt(4)\n"
      "v_mfma_f32_16x16x32_bf16 v[92:95], a[68:71], v[168:171], v[92:95]\n"
      "buffer_load_dwordx4 a[136:139], v5, s[20:23], s82 offen\n"
      "v_mfma_f32_16x16x32_bf16 v[108:111], a[72:75], v[168:171], v[108:111]\n"
      "buffer_load_dwordx4 a[140:143], v5, s[20:23], s82 offen offset:1024\n"
      "v_mfma_f32_16x16x32_bf16 v[136:139], a[76:79], v[168:171], v[136:139]\n"
      "s_waitcnt vmcnt(15)\n"
      "s_waitcnt vmcnt(14)\n"
      "s_waitcnt vmcnt(13)\n"
      "v_lshlrev_b32 v50, 3, v48\n"
      "v_add_u32 v50, 160768, v50\n"
      "ds_read_b64 v[74:75], v50\n"
      "ds_read_b64 v[128:129], v50 offset:128\n"
      "ds_read_b64 v[130:131], v50 offset:256\n"
      "ds_read_b64 v[132:133], v50 offset:384\n"
      "s_waitcnt lgkmcnt(7)\n"
      "v_mfma_f32_16x16x32_bf16 v[76:79], a[80:83], v[140:143], v[76:79]\n"
      "ds_read_b128 v[156:159], v5 offset:36864\n"
      "v_mfma_f32_16x16x32_bf16 v[96:99], a[84:87], v[140:143], v[96:99]\n"
      "buffer_load_dwordx4 a[144:147], v5, s[20:23], s82 offen offset:2048\n"
      "v_mfma_f32_16x16x32_bf16 v[112:115], a[88:91], v[140:143], v[112:115]\n"
      "s_waitcnt lgkmcnt(7)\n"
      "v_mfma_f32_16x16x32_bf16 v[80:83], a[80:83], v[144:147], v[80:83]\n"
      "ds_read_b128 v[160:163], v5 offset:37888\n"
      "v_mfma_f32_16x16x32_bf16 v[100:103], a[84:87], v[144:147], v[100:103]\n"
      "buffer_load_dwordx4 a[148:151], v5, s[20:23], s82 offen offset:3072\n"
      "v_mfma_f32_16x16x32_bf16 v[116:119], a[88:91], v[144:147], v[116:119]\n"
      "s_waitcnt lgkmcnt(7)\n"
      "v_mfma_f32_16x16x32_bf16 v[84:87], a[80:83], v[148:151], v[84:87]\n"
      "ds_read_b128 v[164:167], v5 offset:38912\n"
      "v_mfma_f32_16x16x32_bf16 v[104:107], a[84:87], v[148:151], v[104:107]\n"
      "s_add_u32 s82, s82, 4096\n"
      "v_mfma_f32_16x16x32_bf16 v[120:123], a[88:91], v[148:151], v[120:123]\n"
      "s_waitcnt lgkmcnt(7)\n"
      "v_mfma_f32_16x16x32_bf16 v[92:95], a[80:83], v[152:155], v[92:95]\n"
      "ds_read_b128 v[168:171], v5 offset:39936\n"
      "v_mfma_f32_16x16x32_bf16 v[108:111], a[84:87], v[152:155], v[108:111]\n"
      "buffer_load_dwordx4 a[152:155], v5, s[20:23], s82 offen\n"
      "v_mfma_f32_16x16x32_bf16 v[136:139], a[88:91], v[152:155], v[136:139]\n"
      "s_waitcnt vmcnt(15)\n"
      "s_waitcnt vmcnt(14)\n"
      "s_waitcnt vmcnt(13)\n"
      "s_waitcnt lgkmcnt(4)\n"
      "s_waitcnt lgkmcnt(3)\n"
      "v_mfma_f32_16x16x32_bf16 v[76:79], a[92:95], v[156:159], v[76:79]\n"
      "ds_read_b128 v[140:143], v5 offset:40960\n"
      "buffer_load_dwordx4 a[156:159], v5, s[20:23], s82 offen offset:1024\n"
      "v_mfma_f32_16x16x32_bf16 v[96:99], a[96:99], v[156:159], v[96:99]\n"
      "v_mul_f32 v74, v74, v75\n"
      "v_mfma_f32_16x16x32_bf16 v[112:115], a[100:103], v[156:159], v[112:115]\n"
      "v_mul_f32 v128, v128, v129\n"
      "ds_read_b128 v[144:147], v5 offset:41984\n"
      "s_waitcnt lgkmcnt(4)\n"
      "v_mfma_f32_16x16x32_bf16 v[80:83], a[92:95], v[160:163], v[80:83]\n"
      "buffer_load_dwordx4 a[160:163], v5, s[20:23], s82 offen offset:2048\n"
      "v_mfma_f32_16x16x32_bf16 v[100:103], a[96:99], v[160:163], v[100:103]\n"
      "v_mul_f32 v130, v130, v131\n"
      "v_mul_f32 v132, v132, v133\n"
      "v_mfma_f32_16x16x32_bf16 v[116:119], a[100:103], v[160:163], v[116:119]\n"
      "ds_read_b128 v[148:151], v5 offset:43008\n"
      "s_waitcnt lgkmcnt(4)\n"
      "v_mfma_f32_16x16x32_bf16 v[84:87], a[92:95], v[164:167], v[84:87]\n"
      "buffer_load_dwordx4 a[164:167], v5, s[20:23], s82 offen offset:3072\n"
      "v_mfma_f32_16x16x32_bf16 v[104:107], a[96:99], v[164:167], v[104:107]\n"
      "v_pk_fma_f32 v[56:57], v[74:75], v[176:177], v[172:173] op_sel:[0,0,0] op_sel_hi:[0,1,1] neg_lo:[1,0,0] neg_hi:[1,0,0]\n"
      "v_pk_fma_f32 v[58:59], v[74:75], v[178:179], v[174:175] op_sel:[0,0,0] op_sel_hi:[0,1,1] neg_lo:[1,0,0] neg_hi:[1,0,0]\n"
      "v_mfma_f32_16x16x32_bf16 v[120:123], a[100:103], v[164:167], v[120:123]\n"
      "ds_read_b128 v[152:155], v5 offset:44032\n"
      "s_waitcnt lgkmcnt(4)\n"
      "v_mfma_f32_16x16x32_bf16 v[92:95], a[92:95], v[168:171], v[92:95]\n"
      "s_add_u32 s82, s82, 4096\n"
      "v_pk_fma_f32 v[60:61], v[128:129], v[176:177], v[172:173] op_sel:[0,0,0] op_sel_hi:[0,1,1] neg_lo:[1,0,0] neg_hi:[1,0,0]\n"
      "v_mfma_f32_16x16x32_bf16 v[108:111], a[96:99], v[168:171], v[108:111]\n"
      "v_pk_fma_f32 v[62:63], v[128:129], v[178:179], v[174:175] op_sel:[0,0,0] op_sel_hi:[0,1,1] neg_lo:[1,0,0] neg_hi:[1,0,0]\n"
      "v_mfma_f32_16x16x32_bf16 v[136:139], a[100:103], v[168:171], v[136:139]\n"
      "buffer_load_dwordx4 a[168:171], v5, s[20:23], s82 offen\n"
      "s_waitcnt vmcnt(16)\n"
      "s_waitcnt vmcnt(15)\n"
      "s_waitcnt vmcnt(14)\n"
      "s_waitcnt lgkmcnt(3)\n"
      "v_mfma_f32_16x16x32_bf16 v[76:79], a[104:107], v[140:143], v[76:79]\n"
      "ds_read_b128 v[156:159], v5 offset:45056\n"
      "buffer_load_dwordx4 a[172:175], v5, s[20:23], s82 offen offset:1024\n"
      "v_mfma_f32_16x16x32_bf16 v[96:99], a[108:111], v[140:143], v[96:99]\n"
      "v_pk_fma_f32 v[64:65], v[130:131], v[176:177], v[172:173] op_sel:[0,0,0] op_sel_hi:[0,1,1] neg_lo:[1,0,0] neg_hi:[1,0,0]\n"
      "v_mfma_f32_16x16x32_bf16 v[112:115], a[112:115], v[140:143], v[112:115]\n"
      "v_pk_fma_f32 v[66:67], v[130:131], v[178:179], v[174:175] op_sel:[0,0,0] op_sel_hi:[0,1,1] neg_lo:[1,0,0] neg_hi:[1,0,0]\n"
      "s_waitcnt lgkmcnt(3)\n"
      "v_mfma_f32_16x16x32_bf16 v[80:83], a[104:107], v[144:147], v[80:83]\n"
      "ds_read_b128 v[160:163], v5 offset:46080\n"
      "buffer_load_dwordx4 a[176:179], v5, s[20:23], s82 offen offset:2048\n"
      "v_mfma_f32_16x16x32_bf16 v[100:103], a[108:111], v[144:147], v[100:103]\n"
      "v_pk_fma_f32 v[68:69], v[132:133], v[176:177], v[172:173] op_sel:[0,0,0] op_sel_hi:[0,1,1] neg_lo:[1,0,0] neg_hi:[1,0,0]\n"
      "v_mfma_f32_16x16x32_bf16 v[116:119], a[112:115], v[144:147], v[116:119]\n"
      "v_pk_fma_f32 v[70:71], v[132:133], v[178:179], v[174:175] op_sel:[0,0,0] op_sel_hi:[0,1,1] neg_lo:[1,0,0] neg_hi:[1,0,0]\n"
      "s_waitcnt lgkmcnt(3)\n"
      "v_mfma_f32_16x16x32_bf16 v[84:87], a[104:107], v[148:151], v[84:87]\n"
      "ds_read_b128 v[164:167], v5 offset:47104\n"
      "buffer_load_dwordx4 a[180:183], v5, s[20:23], s82 offen offset:3072\n"
      "v_mfma_f32_16x16x32_bf16 v[104:107], a[108:111], v[148:151], v[104:107]\n"
      "v_pk_fma_f32 v[196:197], v[74:75], v[184:185], v[180:181] op_sel:[0,0,0] op_sel_hi:[0,1,1] neg_lo:[1,0,0] neg_hi:[1,0,0]\n"
      "v_mfma_f32_16x16x32_bf16 v[120:123], a[112:115], v[148:151], v[120:123]\n"
      "v_pk_fma_f32 v[198:199], v[74:75], v[186:187], v[182:183] op_sel:[0,0,0] op_sel_hi:[0,1,1] neg_lo:[1,0,0] neg_hi:[1,0,0]\n"
      "s_waitcnt lgkmcnt(3)\n"
      "v_mfma_f32_16x16x32_bf16 v[92:95], a[104:107], v[152:155], v[92:95]\n"
      "ds_read_b128 v[168:171], v5 offset:48128\n"
      "s_add_u32 s82, s82, 4096\n"
      "v_mfma_f32_16x16x32_bf16 v[108:111], a[108:111], v[152:155], v[108:111]\n"
      "v_pk_fma_f32 v[200:201], v[128:129], v[184:185], v[180:181] op_sel:[0,0,0] op_sel_hi:[0,1,1] neg_lo:[1,0,0] neg_hi:[1,0,0]\n"
      "v_mfma_f32_16x16x32_bf16 v[136:139], a[112:115], v[152:155], v[136:139]\n"
      "v_pk_fma_f32 v[202:203], v[128:129], v[186:187], v[182:183] op_sel:[0,0,0] op_sel_hi:[0,1,1] neg_lo:[1,0,0] neg_hi:[1,0,0]\n"
      "s_waitcnt vmcnt(16)\n"
      "s_waitcnt vmcnt(15)\n"
      "s_waitcnt vmcnt(14)\n"
      "s_barrier\n"
      "s_waitcnt lgkmcnt(3)\n"
      "v_mfma_f32_16x16x32_bf16 v[76:79], a[116:119], v[156:159], v[76:79]\n"
      "ds_read_b128 v[140:143], v5 offset:49152\n"
      "buffer_load_dwordx4 a[184:187], v5, s[20:23], s82 offen\n"
      "v_mfma_f32_16x16x32_bf16 v[96:99], a[120:123], v[156:159], v[96:99]\n"
      "v_pk_fma_f32 v[204:205], v[130:131], v[184:185], v[180:181] op_sel:[0,0,0] op_sel_hi:[0,1,1] neg_lo:[1,0,0] neg_hi:[1,0,0]\n"
      "v_mfma_f32_16x16x32_bf16 v[112:115], a[124:127], v[156:159], v[112:115]\n"
      "v_pk_fma_f32 v[206:207], v[130:131], v[186:187], v[182:183] op_sel:[0,0,0] op_sel_hi:[0,1,1] neg_lo:[1,0,0] neg_hi:[1,0,0]\n"
      "ds_read_b128 v[144:147], v5 offset:50176\n"
      "s_waitcnt lgkmcnt(4)\n"
      "v_mfma_f32_16x16x32_bf16 v[80:83], a[116:119], v[160:163], v[80:83]\n"
      "buffer_load_dwordx4 a[188:191], v5, s[20:23], s82 offen offset:1024\n"
      "v_mfma_f32_16x16x32_bf16 v[100:103], a[120:123], v[160:163], v[100:103]\n"
      "v_pk_fma_f32 v[208:209], v[132:133], v[184:185], v[180:181] op_sel:[0,0,0] op_sel_hi:[0,1,1] neg_lo:[1,0,0] neg_hi:[1,0,0]\n"
      "v_pk_fma_f32 v[210:211], v[132:133], v[186:187], v[182:183] op_sel:[0,0,0] op_sel_hi:[0,1,1] neg_lo:[1,0,0] neg_hi:[1,0,0]\n"
      "v_mfma_f32_16x16x32_bf16 v[116:119], a[124:127], v[160:163], v[116:119]\n"
      "ds_read_b128 v[148:151], v5 offset:51200\n"
      "s_waitcnt lgkmcnt(4)\n"
      "v_mfma_f32_16x16x32_bf16 v[84:87], a[116:119], v[164:167], v[84:87]\n"
      "buffer_load_dwordx4 a[192:195], v5, s[20:23], s82 offen offset:2048\n"
      "v_mfma_f32_16x16x32_bf16 v[104:107], a[120:123], v[164:167], v[104:107]\n"
      "v_pk_fma_f32 v[212:213], v[74:75], v[192:193], v[188:189] op_sel:[0,0,0] op_sel_hi:[0,1,1] neg_lo:[1,0,0] neg_hi:[1,0,0]\n"
      "v_pk_fma_f32 v[214:215], v[74:75], v[194:195], v[190:191] op_sel:[0,0,0] op_sel_hi:[0,1,1] neg_lo:[1,0,0] neg_hi:[1,0,0]\n"
      "v_mfma_f32_16x16x32_bf16 v[120:123], a[124:127], v[164:167], v[120:123]\n"
      "ds_read_b128 v[152:155], v5 offset:52224\n"
      "s_waitcnt lgkmcnt(4)\n"
      "v_mfma_f32_16x16x32_bf16 v[92:95], a[116:119], v[168:171], v[92:95]\n"
      "buffer_load_dwordx4 a[196:199], v5, s[20:23], s82 offen offset:3072\n"
      "v_pk_fma_f32 v[216:217], v[128:129], v[192:193], v[188:189] op_sel:[0,0,0] op_sel_hi:[0,1,1] neg_lo:[1,0,0] neg_hi:[1,0,0]\n"
      "v_mfma_f32_16x16x32_bf16 v[108:111], a[120:123], v[168:171], v[108:111]\n"
      "v_pk_fma_f32 v[218:219], v[128:129], v[194:195], v[190:191] op_sel:[0,0,0] op_sel_hi:[0,1,1] neg_lo:[1,0,0] neg_hi:[1,0,0]\n"
      "v_mfma_f32_16x16x32_bf16 v[136:139], a[124:127], v[168:171], v[136:139]\n"
      "s_add_u32 s82, s82, 4096\n"
      "s_waitcnt vmcnt(17)\n"
      "s_waitcnt vmcnt(16)\n"
      "s_waitcnt vmcnt(15)\n"
      "s_waitcnt lgkmcnt(3)\n"
      "v_mfma_f32_16x16x32_bf16 v[76:79], a[128:131], v[140:143], v[76:79]\n"
      "ds_read_b128 v[156:159], v5 offset:53248\n"
      "v_mfma_f32_16x16x32_bf16 v[96:99], a[132:135], v[140:143], v[96:99]\n"
      "buffer_load_dwordx4 a[200:203], v5, s[20:23], s82 offen\n"
      "v_mfma_f32_16x16x32_bf16 v[112:115], a[136:139], v[140:143], v[112:115]\n"
      "v_pk_fma_f32 v[220:221], v[130:131], v[192:193], v[188:189] op_sel:[0,0,0] op_sel_hi:[0,1,1] neg_lo:[1,0,0] neg_hi:[1,0,0]\n"
      "s_waitcnt lgkmcnt(3)\n"
      "v_mfma_f32_16x16x32_bf16 v[80:83], a[128:131], v[144:147], v[80:83]\n"
      "v_pk_fma_f32 v[222:223], v[130:131], v[194:195], v[190:191] op_sel:[0,0,0] op_sel_hi:[0,1,1] neg_lo:[1,0,0] neg_hi:[1,0,0]\n"
      "v_mfma_f32_16x16x32_bf16 v[100:103], a[132:135], v[144:147], v[100:103]\n"
      "ds_read_b128 v[160:163], v5 offset:54272\n"
      "v_mfma_f32_16x16x32_bf16 v[116:119], a[136:139], v[144:147], v[116:119]\n"
      "buffer_load_dwordx4 a[204:207], v5, s[20:23], s82 offen offset:1024\n"
      "s_waitcnt lgkmcnt(3)\n"
      "v_mfma_f32_16x16x32_bf16 v[84:87], a[128:131], v[148:151], v[84:87]\n"
      "v_pk_fma_f32 v[224:225], v[132:133], v[192:193], v[188:189] op_sel:[0,0,0] op_sel_hi:[0,1,1] neg_lo:[1,0,0] neg_hi:[1,0,0]\n"
      "v_mfma_f32_16x16x32_bf16 v[104:107], a[132:135], v[148:151], v[104:107]\n"
      "v_pk_fma_f32 v[226:227], v[132:133], v[194:195], v[190:191] op_sel:[0,0,0] op_sel_hi:[0,1,1] neg_lo:[1,0,0] neg_hi:[1,0,0]\n"
      "v_mfma_f32_16x16x32_bf16 v[120:123], a[136:139], v[148:151], v[120:123]\n"
      "ds_read_b128 v[164:167], v5 offset:55296\n"
      "s_waitcnt lgkmcnt(3)\n"
      "v_mfma_f32_16x16x32_bf16 v[92:95], a[128:131], v[152:155], v[92:95]\n"
      "buffer_load_dwordx4 a[208:211], v5, s[20:23], s82 offen offset:2048\n"
      "v_mfma_f32_16x16x32_bf16 v[108:111], a[132:135], v[152:155], v[108:111]\n"
      "ds_read_b128 v[168:171], v5 offset:56320\n"
      "v_mfma_f32_16x16x32_bf16 v[136:139], a[136:139], v[152:155], v[136:139]\n"
      "buffer_load_dwordx4 a[212:215], v5, s[20:23], s82 offen offset:3072\n"
      "s_waitcnt vmcnt(18)\n"
      "s_waitcnt vmcnt(17)\n"
      "s_waitcnt vmcnt(16)\n"
      "s_waitcnt lgkmcnt(3)\n"
      "v_mfma_f32_16x16x32_bf16 v[76:79], a[140:143], v[156:159], v[76:79]\n"
      "ds_read_b128 v[140:143], v5 offset:57344\n"
      "v_mfma_f32_16x16x32_bf16 v[96:99], a[144:147], v[156:159], v[96:99]\n"
      "s_add_u32 s82, s82, 4096\n"
      "v_mfma_f32_16x16x32_bf16 v[112:115], a[148:151], v[156:159], v[112:115]\n"
      "ds_read_b128 v[144:147], v5 offset:58368\n"
      "s_waitcnt lgkmcnt(4)\n"
      "v_mfma_f32_16x16x32_bf16 v[80:83], a[140:143], v[160:163], v[80:83]\n"
      "v_mfma_f32_16x16x32_bf16 v[100:103], a[144:147], v[160:163], v[100:103]\n"
      "buffer_load_dwordx4 a[216:219], v5, s[20:23], s82 offen\n"
      "v_mfma_f32_16x16x32_bf16 v[116:119], a[148:151], v[160:163], v[116:119]\n"
      "ds_read_b128 v[148:151], v5 offset:59392\n"
      "s_waitcnt lgkmcnt(4)\n"
      "v_mfma_f32_16x16x32_bf16 v[84:87], a[140:143], v[164:167], v[84:87]\n"
      "buffer_load_dwordx4 a[220:223], v5, s[20:23], s82 offen offset:1024\n"
      "v_mfma_f32_16x16x32_bf16 v[104:107], a[144:147], v[164:167], v[104:107]\n"
      "v_mfma_f32_16x16x32_bf16 v[120:123], a[148:151], v[164:167], v[120:123]\n"
      "ds_read_b128 v[152:155], v5 offset:60416\n"
      "s_waitcnt lgkmcnt(4)\n"
      "v_mfma_f32_16x16x32_bf16 v[92:95], a[140:143], v[168:171], v[92:95]\n"
      "buffer_load_dwordx4 a[224:227], v5, s[20:23], s82 offen offset:2048\n"
      "v_mfma_f32_16x16x32_bf16 v[108:111], a[144:147], v[168:171], v[108:111]\n"
      "buffer_load_dwordx4 a[228:231], v5, s[20:23], s82 offen offset:3072\n"
      "v_mfma_f32_16x16x32_bf16 v[136:139], a[148:151], v[168:171], v[136:139]\n"
      "s_waitcnt vmcnt(19)\n"
      "s_waitcnt vmcnt(18)\n"
      "s_waitcnt vmcnt(17)\n"
      "s_waitcnt lgkmcnt(3)\n"
      "v_mfma_f32_16x16x32_bf16 v[76:79], a[152:155], v[140:143], v[76:79]\n"
      "ds_read_b128 v[156:159], v5 offset:61440\n"
      "v_mfma_f32_16x16x32_bf16 v[96:99], a[156:159], v[140:143], v[96:99]\n"
      "s_add_u32 s82, s82, 4096\n"
      "v_mfma_f32_16x16x32_bf16 v[112:115], a[160:163], v[140:143], v[112:115]\n"
      "s_waitcnt lgkmcnt(3)\n"
      "v_mfma_f32_16x16x32_bf16 v[80:83], a[152:155], v[144:147], v[80:83]\n"
      "ds_read_b128 v[160:163], v5 offset:62464\n"
      "v_mfma_f32_16x16x32_bf16 v[100:103], a[156:159], v[144:147], v[100:103]\n"
      "buffer_load_dwordx4 a[232:235], v5, s[20:23], s82 offen\n"
      "v_mfma_f32_16x16x32_bf16 v[116:119], a[160:163], v[144:147], v[116:119]\n"
      "s_waitcnt lgkmcnt(3)\n"
      "v_mfma_f32_16x16x32_bf16 v[84:87], a[152:155], v[148:151], v[84:87]\n"
      "ds_read_b128 v[164:167], v5 offset:63488\n"
      "v_mfma_f32_16x16x32_bf16 v[104:107], a[156:159], v[148:151], v[104:107]\n"
      "buffer_load_dwordx4 a[236:239], v5, s[20:23], s82 offen offset:1024\n"
      "v_mfma_f32_16x16x32_bf16 v[120:123], a[160:163], v[148:151], v[120:123]\n"
      "s_waitcnt lgkmcnt(3)\n"
      "v_mfma_f32_16x16x32_bf16 v[92:95], a[152:155], v[152:155], v[92:95]\n"
      "ds_read_b128 v[168:171], v5 offset:64512\n"
      "v_mfma_f32_16x16x32_bf16 v[108:111], a[156:159], v[152:155], v[108:111]\n"
      "buffer_load_dwordx4 a[240:243], v5, s[20:23], s82 offen offset:2048\n"
      "v_mfma_f32_16x16x32_bf16 v[136:139], a[160:163], v[152:155], v[136:139]\n"
      "s_waitcnt vmcnt(19)\n"
      "s_waitcnt vmcnt(18)\n"
      "s_waitcnt vmcnt(17)\n"
      "s_waitcnt lgkmcnt(3)\n"
      "v_mfma_f32_16x16x32_bf16 v[76:79], a[164:167], v[156:159], v[76:79]\n"
      "buffer_load_dwordx4 a[244:247], v5, s[20:23], s82 offen offset:3072\n"
      "v_mfma_f32_16x16x32_bf16 v[96:99], a[168:171], v[156:159], v[96:99]\n"
      "v_mfma_f32_16x16x32_bf16 v[112:115], a[172:175], v[156:159], v[112:115]\n"
      "s_add_u32 s82, s82, 4096\n"
      "s_waitcnt lgkmcnt(2)\n"
      "v_mfma_f32_16x16x32_bf16 v[80:83], a[164:167], v[160:163], v[80:83]\n"
      "v_mfma_f32_16x16x32_bf16 v[100:103], a[168:171], v[160:163], v[100:103]\n"
      "buffer_load_dwordx4 a[248:251], v5, s[20:23], s82 offen\n"
      "v_mfma_f32_16x16x32_bf16 v[116:119], a[172:175], v[160:163], v[116:119]\n"
      "s_waitcnt lgkmcnt(1)\n"
      "v_mfma_f32_16x16x32_bf16 v[84:87], a[164:167], v[164:167], v[84:87]\n"
      "v_mfma_f32_16x16x32_bf16 v[104:107], a[168:171], v[164:167], v[104:107]\n"
      "buffer_load_dwordx4 a[252:255], v5, s[20:23], s82 offen offset:1024\n"
      "v_mfma_f32_16x16x32_bf16 v[120:123], a[172:175], v[164:167], v[120:123]\n"
      "s_waitcnt lgkmcnt(0)\n"
      "v_mfma_f32_16x16x32_bf16 v[92:95], a[164:167], v[168:171], v[92:95]\n"
      "buffer_load_dwordx4 v[8:11], v5, s[20:23], s82 offen offset:2048\n"
      "v_mfma_f32_16x16x32_bf16 v[108:111], a[168:171], v[168:171], v[108:111]\n"
      "v_mfma_f32_16x16x32_bf16 v[136:139], a[172:175], v[168:171], v[136:139]\n"
      "; P3 epilogue\n"
      "s_mul_i32 s70, s28, 225280\n"
      "s_add_u32 s70, s70, 11100160\n"
      "s_mul_i32 s71, s29, 11\n"
      "s_mul_i32 s83, s30, 3\n"
      "s_add_u32 s71, s71, s83\n"
      "s_add_u32 s83, s71, 0\n"
      "s_lshr_b32 s84, s83, 1\n"
      "s_mul_i32 s84, s84, 5120\n"
      "s_and_b32 s85, s83, 1\n"
      "s_lshl_b32 s85, s85, 9\n"
      "s_add_u32 s84, s84, s85\n"
      "s_add_u32 s84, s84, s70\n"
      "s_cmp_lt_u32 s83, 86\n"
      "s_cselect_b32 s84, s84, 0x7ff00000\n"
      "s_mov_b32 s72, s84\n"
      "s_add_u32 s83, s71, 1\n"
      "s_lshr_b32 s84, s83, 1\n"
      "s_mul_i32 s84, s84, 5120\n"
      "s_and_b32 s85, s83, 1\n"
      "s_lshl_b32 s85, s85, 9\n"
      "s_add_u32 s84, s84, s85\n"
      "s_add_u32 s84, s84, s70\n"
      "s_cmp_lt_u32 s83, 86\n"
      "s_cselect_b32 s84, s84, 0x7ff00000\n"
      "s_mov_b32 s73, s84\n"
      "s_add_u32 s83, s71, 2\n"
      "s_lshr_b32 s84, s83, 1\n"
      "s_mul_i32 s84, s84, 5120\n"
      "s_and_b32 s85, s83, 1\n"
      "s_lshl_b32 s85, s85, 9\n"
      "s_add_u32 s84, s84, s85\n"
      "s_add_u32 s84, s84, s70\n"
      "s_cmp_lt_u32 s83, 86\n"
      "s_cselect_b32 s84, s84, 0x7ff00000\n"
      "s_cmp_lt_u32 s30, 3\n"
      "s_cselect_b32 s84, s84, 0x7ff00000\n"
      "s_mov_b32 s74, s84\n"
      "v_pk_fma_f32 v[76:77], v[74:75], v[76:77], v[56:57] op_sel:[1,0,0] op_sel_hi:[1,1,1]\n"
      "buffer_load_dwordx4 v[12:15], v5, s[20:23], s82 offen offset:3072\n"
      "v_pk_fma_f32 v[78:79], v[74:75], v[78:79], v[58:59] op_sel:[1,0,0] op_sel_hi:[1,1,1]\n"
      "v_pk_fma_f32 v[80:81], v[128:129], v[80:81], v[60:61] op_sel:[1,0,0] op_sel_hi:[1,1,1]\n"
      "v_pk_fma_f32 v[82:83], v[128:129], v[82:83], v[62:63] op_sel:[1,0,0] op_sel_hi:[1,1,1]\n"
      "v_pk_fma_f32 v[84:85], v[130:131], v[84:85], v[64:65] op_sel:[1,0,0] op_sel_hi:[1,1,1]\n"
      "v_pk_fma_f32 v[86:87], v[130:131], v[86:87], v[66:67] op_sel:[1,0,0] op_sel_hi:[1,1,1]\n"
      "v_pk_fma_f32 v[92:93], v[132:133], v[92:93], v[68:69] op_sel:[1,0,0] op_sel_hi:[1,1,1]\n"
      "v_pk_fma_f32 v[94:95], v[132:133], v[94:95], v[70:71] op_sel:[1,0,0] op_sel_hi:[1,1,1]\n"
      "v_cvt_pk_bf16_f32 v140, v76, v77\n"
      "v_cvt_pk_bf16_f32 v141, v78, v79\n"
      "v_cvt_pk_bf16_f32 v142, v80, v81\n"
      "v_cvt_pk_bf16_f32 v143, v82, v83\n"
      "v_cvt_pk_bf16_f32 v144, v84, v85\n"
      "v_cvt_pk_bf16_f32 v145, v86, v87\n"
      "v_cvt_pk_bf16_f32 v146, v92, v93\n"
      "v_cvt_pk_bf16_f32 v147, v94, v95\n"
      "v_pk_max_i16 v140, v140, 0\n"
      "v_pk_max_i16 v141, v141, 0\n"
      "v_pk_max_i16 v142, v142, 0\n"
      "s_add_u32 s82, s82, 4096\n"
      "buffer_load_dwordx4 v[16:19], v5, s[20:23], s82 offen\n"
      "v_pk_max_i16 v143, v143, 0\n"
      "v_pk_max_i16 v144, v144, 0\n"
      "v_pk_max_i16 v145, v145, 0\n"
      "v_pk_max_i16 v146, v146, 0\n"
      "v_pk_max_i16 v147, v147, 0\n"
      "v_permlane16_swap_b32 v140, v142\n"
      "v_permlane16_swap_b32 v141, v143\n"
      "v_permlane16_swap_b32 v144, v146\n"
      "v_permlane16_swap_b32 v145, v147\n"
      "buffer_store_dwordx4 v[140:143], v6, s[20:23], s72 offen sc1\n"
      "buffer_store_dwordx4 v[144:147], v6, s[20:23], s72 offen offset:2048 sc1\n"
      "v_pk_fma_f32 v[96:97], v[74:75], v[96:97], v[196:197] op_sel:[1,0,0] op_sel_hi:[1,1,1]\n"
      "v_pk_fma_f32 v[98:99], v[74:75], v[98:99], v[198:199] op_sel:[1,0,0] op_sel_hi:[1,1,1]\n"
      "v_pk_fma_f32 v[100:101], v[128:129], v[100:101], v[200:201] op_sel:[1,0,0] op_sel_hi:[1,1,1]\n"
      "v_pk_fma_f32 v[102:103], v[128:129], v[102:103], v[202:203] op_sel:[1,0,0] op_sel_hi:[1,1,1]\n"
      "v_pk_fma_f32 v[104:105], v[130:131], v[104:105], v[204:205] op_sel:[1,0,0] op_sel_hi:[1,1,1]\n"
      "v_pk_fma_f32 v[106:107], v[130:131], v[106:107], v[206:207] op_sel:[1,0,0] op_sel_hi:[1,1,1]\n"
      "v_pk_fma_f32 v[108:109], v[132:133], v[108:109], v[208:209] op_sel:[1,0,0] op_sel_hi:[1,1,1]\n"
      "v_pk_fma_f32 v[110:111], v[132:133], v[110:111], v[210:211] op_sel:[1,0,0] op_sel_hi:[1,1,1]\n"
      "v_cvt_pk_bf16_f32 v148, v96, v97\n"
      "v_cvt_pk_bf16_f32 v149, v98, v99\n"
      "v_cvt_pk_bf16_f32 v150, v100, v101\n"
      "v_cvt_pk_bf16_f32 v151, v102, v103\n"
      "v_cvt_pk_bf16_f32 v152, v104, v105\n"
      "v_cvt_pk_bf16_f32 v153, v106, v107\n"
      "v_cvt_pk_bf16_f32 v154, v108, v109\n"
      "v_cvt_pk_bf16_f32 v155, v110, v111\n"
      "v_pk_max_i16 v148, v148, 0\n"
      "v_pk_max_i16 v149, v149, 0\n"
      "buffer_load_dwordx4 v[20:23], v5, s[20:23], s82 offen offset:1024\n"
      "v_pk_max_i16 v150, v150, 0\n"
      "v_pk_max_i16 v151, v151, 0\n"
      "v_pk_max_i16 v152, v152, 0\n"
      "v_pk_max_i16 v153, v153, 0\n"
      "v_pk_max_i16 v154, v154, 0\n"
      "v_pk_max_i16 v155, v155, 0\n"
      "v_permlane16_swap_b32 v148, v150\n"
      "v_permlane16_swap_b32 v149, v151\n"
      "v_permlane16_swap_b32 v152, v154\n"
      "v_permlane16_swap_b32 v153, v155\n"
      "buffer_store_dwordx4 v[148:151], v6, s[20:23], s73 offen sc1\n"
      "buffer_store_dwordx4 v[152:155], v6, s[20:23], s73 offen offset:2048 sc1\n"
      "v_pk_fma_f32 v[112:113], v[74:75], v[112:113], v[212:213] op_sel:[1,0,0] op_sel_hi:[1,1,1]\n"
      "v_pk_fma_f32 v[114:115], v[74:75], v[114:115], v[214:215] op_sel:[1,0,0] op_sel_hi:[1,1,1]\n"
      "v_pk_fma_f32 v[116:117], v[128:129], v[116:117], v[216:217] op_sel:[1,0,0] op_sel_hi:[1,1,1]\n"
      "v_pk_fma_f32 v[118:119], v[128:129], v[118:119], v[218:219] op_sel:[1,0,0] op_sel_hi:[1,1,1]\n"
      "v_pk_fma_f32 v[120:121], v[130:131], v[120:121], v[220:221] op_sel:[1,0,0] op_sel_hi:[1,1,1]\n"
      "v_pk_fma_f32 v[122:123], v[130:131], v[122:123], v[222:223] op_sel:[1,0,0] op_sel_hi:[1,1,1]\n"
      "v_pk_fma_f32 v[136:137], v[132:133], v[136:137], v[224:225] op_sel:[1,0,0] op_sel_hi:[1,1,1]\n"
      "v_pk_fma_f32 v[138:139], v[132:133], v[138:139], v[226:227] op_sel:[1,0,0] op_sel_hi:[1,1,1]\n"
      "v_cvt_pk_bf16_f32 v156, v112, v113\n"
      "v_cvt_pk_bf16_f32 v157, v114, v115\n"
      "v_cvt_pk_bf16_f32 v158, v116, v117\n"
      "v_cvt_pk_bf16_f32 v159, v118, v119\n"
      "v_cvt_pk_bf16_f32 v160, v120, v121\n"
      "v_cvt_pk_bf16_f32 v161, v122, v123\n"
      "v_cvt_pk_bf16_f32 v162, v136, v137\n"
      "v_cvt_pk_bf16_f32 v163, v138, v139\n"
      "v_pk_max_i16 v156, v156, 0\n"
      "v_pk_max_i16 v157, v157, 0\n"
      "v_pk_max_i16 v158, v158, 0\n"
      "v_pk_max_i16 v159, v159, 0\n"
      "v_pk_max_i16 v160, v160, 0\n"
      "v_pk_max_i16 v161, v161, 0\n"
      "v_pk_max_i16 v162, v162, 0\n"
      "v_pk_max_i16 v163, v163, 0\n"
      "v_permlane16_swap_b32 v156, v158\n"
      "v_permlane16_swap_b32 v157, v159\n"
      "v_permlane16_swap_b32 v160, v162\n"
      "v_permlane16_swap_b32 v161, v163\n"
      "buffer_store_dwordx4 v[156:159], v6, s[20:23], s74 offen sc1\n"
      "buffer_store_dwordx4 v[160:163], v6, s[20:23], s74 offen offset:2048 sc1\n"
      "; X3\n"
      "buffer_load_dwordx4 v[24:27], v5, s[20:23], s82 offen offset:2048\n"
      "buffer_load_dwordx4 v[28:31], v5, s[20:23], s82 offen offset:3072\n"
      "s_add_u32 s82, s82, 4096\n"
      "buffer_load_dwordx4 v[32:35], v5, s[20:23], s82 offen\n"
      "buffer_load_dwordx4 v[36:39], v5, s[20:23], s82 offen offset:1024\n"
      "buffer_load_dwordx4 v[40:43], v5, s[20:23], s82 offen offset:2048\n"
      "buffer_load_dwordx4 v[44:47], v5, s[20:23], s82 offen offset:3072\n"
      "s_waitcnt vmcnt(6)\n"
      "s_barrier\n"
      "s_cmp_lg_u32 s30, 0\n"
      "s_cbranch_scc1 Lpub2_%=\n"
      "s_lshl_b32 s83, s28, 7\n"
      "s_lshl_b32 s84, s29, 2\n"
      "s_add_u32 s83, s83, s84\n"
      "s_add_u32 s83, s83, 5308416\n"
      "v_mov_b32 v56, 0\n"
      "v_mov_b32 v57, s31\n"
      "s_mov_b64 exec, 1\n"
      "buffer_store_dword v57, v56, s[20:23], s83 offen sc1\n"
      "s_mov_b64 exec, -1\n"
      "v_and_b32 v56, 7, v7\n"
      "v_lshlrev_b32 v56, 2, v56\n"
      "s_lshl_b32 s83, s28, 7\n"
      "s_add_u32 s83, s83, 5308416\n"
      "s_mov_b32 s86, 0\n"
      "s_nop 0\n"
      "Lpoll3_%=:\n"
      "buffer_load_dword v57, v56, s[20:23], s83 offen sc1\n"
      "s_add_u32 s86, s86, 1\n"
      "s_waitcnt vmcnt(0)\n"
      "v_cmp_ne_u32 vcc, s31, v57\n"
      "s_cmp_gt_u32 s86, 0x10000\n"
      "s_cbranch_scc1 Lpub2_%=\n"
      "s_nop 3\n"
      "s_cbranch_vccnz Lpoll3_%=\n"
      "s_nop 0\n"
      "Lpub2_%=:\n"
      "s_barrier\n"
      "s_mul_i32 s84, s30, 5120\n"
      "s_add_u32 s87, s70, s84\n"
      "s_lshl_b32 s84, s30, 12\n"
      "s_add_u32 m0, s84, 0\n"
      "s_nop 0\n"
      "buffer_load_dwordx4 v5, s[20:23], s87 offen sc1 lds\n"
      "buffer_load_dwordx4 v5, s[20:23], s87 offen offset:1024 sc1 lds\n"
      "buffer_load_dwordx4 v5, s[20:23], s87 offen offset:2048 sc1 lds\n"
      "buffer_load_dwordx4 v5, s[20:23], s87 offen offset:3072 sc1 lds\n"
      "s_add_u32 m0, m0, 16384\n"
      "s_add_u32 s87, s87, 20480\n"
      "buffer_load_dwordx4 v5, s[20:23], s87 offen sc1 lds\n"
      "buffer_load_dwordx4 v5, s[20:23], s87 offen offset:1024 sc1 lds\n"
      "buffer_load_dwordx4 v5, s[20:23], s87 offen offset:2048 sc1 lds\n"
      "buffer_load_dwordx4 v5, s[20:23], s87 offen offset:3072 sc1 lds\n"
      "; P4\n"
      "v_add_u32 v56, 65536, v5\n"
      "v_add_u32 v57, 131072, v5\n"
      "s_waitcnt vmcnt(4)\n"
      "s_barrier\n"
      "ds_read_b128 v[76:79], v5\n"
      "ds_read_b128 v[80:83], v5 offset:1024\n"
      "ds_read_b128 v[84:87], v5 offset:2048\n"
      "ds_read_b128 v[92:95], v5 offset:3072\n"
      "ds_read_b128 v[96:99], v5 offset:4096\n"
      "ds_read_b128 v[100:103], v5 offset:5120\n"
      "ds_read_b128 v[104:107], v5 offset:6144\n"
      "ds_read_b128 v[108:111], v5 offset:7168\n"
      "s_waitcnt lgkmcnt(4)\n"
      "v_mfma_f32_16x16x32_bf16 v[60:63], a[176:179], v[76:79], 0\n"
      "ds_read_b128 v[112:115], v5 offset:8192\n"
      "s_add_u32 m0, m0, 16384\n"
      "v_mfma_f32_16x16x32_bf16 v[64:67], a[176:179], v[80:83], 0\n"
      "ds_read_b128 v[116:119], v5 offset:9216\n"
      "s_add_u32 s87, s87, 20480\n"
      "v_mfma_f32_16x16x32_bf16 v[68:71], a[176:179], v[84:87], 0\n"
      "ds_read_b128 v[120:123], v5 offset:10240\n"
      "buffer_load_dwordx4 v5, s[20:23], s87 offen sc1 lds\n"
      "v_mfma_f32_16x16x32_bf16 v[72:75], a[176:179], v[92:95], 0\n"
      "ds_read_b128 v[128:131], v5 offset:11264\n"
      "s_waitcnt lgkmcnt(4)\n"
      "v_mfma_f32_16x16x32_bf16 v[60:63], a[180:183], v[96:99], v[60:63]\n"
      "ds_read_b128 v[76:79], v5 offset:12288\n"
      "buffer_load_dwordx4 v5, s[20:23], s87 offen offset:1024 sc1 lds\n"
      "v_mfma_f32_16x16x32_bf16 v[64:67], a[180:183], v[100:103], v[64:67]\n"
      "ds_read_b128 v[80:83], v5 offset:13312\n"
      "buffer_load_dwordx4 v5, s[20:23], s87 offen offset:2048 sc1 lds\n"
      "v_mfma_f32_16x16x32_bf16 v[68:71], a[180:183], v[104:107], v[68:71]\n"
      "ds_read_b128 v[84:87], v5 offset:14336\n"
      "buffer_load_dwordx4 v5, s[20:23], s87 offen offset:3072 sc1 lds\n"
      "v_mfma_f32_16x16x32_bf16 v[72:75], a[180:183], v[108:111], v[72:75]\n"
      "ds_read_b128 v[92:95], v5 offset:15360\n"
      "s_add_u32 m0, m0, 16384\n"
      "s_waitcnt vmcnt(4)\n"
      "s_barrier\n"
      "s_waitcnt lgkmcnt(4)\n"
      "v_mfma_f32_16x16x32_bf16 v[60:63], a[184:187], v[112:115], v[60:63]\n"
      "ds_read_b128 v[96:99], v5 offset:16384\n"
      "s_add_u32 s87, s87, 20480\n"
      "v_mfma_f32_16x16x32_bf16 v[64:67], a[184:187], v[116:119], v[64:67]\n"
      "ds_read_b128 v[100:103], v5 offset:17408\n"
      "buffer_load_dwordx4 v5, s[20:23], s87 offen sc1 lds\n"
      "v_mfma_f32_16x16x32_bf16 v[68:71], a[184:187], v[120:123], v[68:71]\n"
      "ds_read_b128 v[104:107], v5 offset:18432\n"
      "buffer_load_dwordx4 v5, s[20:23], s87 offen offset:1024 sc1 lds\n"
      "v_mfma_f32_16x16x32_bf16 v[72:75], a[184:187], v[128:131], v[72:75]\n"
      "ds_read_b128 v[108:111], v5 offset:19456\n"
      "s_waitcnt lgkmcnt(4)\n"
      "v_mfma_f32_16x16x32_bf16 v[60:63], a[188:191], v[76:79], v[60:63]\n"
      "ds_read_b128 v[112:115], v5 offset:20480\n"
      "buffer_load_dwordx4 v5, s[20:23], s87 offen offset:2048 sc1 lds\n"
      "v_mfma_f32_16x16x32_bf16 v[64:67], a[188:191], v[80:83], v[64:67]\n"
      "ds_read_b128 v[116:119], v5 offset:21504\n"
      "buffer_load_dwordx4 v5, s[20:23], s87 offen offset:3072 sc1 lds\n"
      "v_mfma_f32_16x16x32_bf16 v[68:71], a[188:191], v[84:87], v[68:71]\n"
      "ds_read_b128 v[120:123], v5 offset:22528\n"
      "s_add_u32 m0, m0, 16384\n"
      "v_mfma_f32_16x16x32_bf16 v[72:75], a[188:191], v[92:95], v[72:75]\n"
      "ds_read_b128 v[128:131], v5 offset:23552\n"
      "s_add_u32 s87, s87, 20480\n"
      "s_waitcnt lgkmcnt(4)\n"
      "v_mfma_f32_16x16x32_bf16 v[60:63], a[192:195], v[96:99], v[60:63]\n"
      "ds_read_b128 v[76:79], v5 offset:24576\n"
      "buffer_load_dwordx4 v5, s[20:23], s87 offen sc1 lds\n"
      "v_mfma_f32_16x16x32_bf16 v[64:67], a[192:195], v[100:103], v[64:67]\n"
      "ds_read_b128 v[80:83], v5 offset:25600\n"
      "buffer_load_dwordx4 v5, s[20:23], s87 offen offset:1024 sc1 lds\n"
      "v_mfma_f32_16x16x32_bf16 v[68:71], a[192:195], v[104:107], v[68:71]\n"
      "ds_read_b128 v[84:87], v5 offset:26624\n"
      "buffer_load_dwordx4 v5, s[20:23], s87 offen offset:2048 sc1 lds\n"
      "v_mfma_f32_16x16x32_bf16 v[72:75], a[192:195], v[108:111], v[72:75]\n"
      "ds_read_b128 v[92:95], v5 offset:27648\n"
      "s_waitcnt lgkmcnt(4)\n"
      "v_mfma_f32_16x16x32_bf16 v[60:63], a[196:199], v[112:115], v[60:63]\n"
      "ds_read_b128 v[96:99], v5 offset:28672\n"
      "buffer_load_dwordx4 v5, s[20:23], s87 offen offset:3072 sc1 lds\n"
      "v_mfma_f32_16x16x32_bf16 v[64:67], a[196:199], v[116:119], v[64:67]\n"
      "ds_read_b128 v[100:103], v5 offset:29696\n"
      "s_add_u32 m0, m0, 16384\n"
      "v_mfma_f32_16x16x32_bf16 v[68:71], a[196:199], v[120:123], v[68:71]\n"
      "ds_read_b128 v[104:107], v5 offset:30720\n"
      "s_add_u32 s87, s87, 20480\n"
      "v_mfma_f32_16x16x32_bf16 v[72:75], a[196:199], v[128:131], v[72:75]\n"
      "ds_read_b128 v[108:111], v5 offset:31744\n"
      "buffer_load_dwordx4 v5, s[20:23], s87 offen sc1 lds\n"
      "s_waitcnt vmcnt(9)\n"
      "s_barrier\n"
      "s_waitcnt lgkmcnt(4)\n"
      "v_mfma_f32_16x16x32_bf16 v[60:63], a[200:203], v[76:79], v[60:63]\n"
      "ds_read_b128 v[112:115], v5 offset:32768\n"
      "buffer_load_dwordx4 v5, s[20:23], s87 offen offset:1024 sc1 lds\n"
      "v_mfma_f32_16x16x32_bf16 v[64:67], a[200:203], v[80:83], v[64:67]\n"
      "ds_read_b128 v[116:119], v5 offset:33792\n"
      "buffer_load_dwordx4 v5, s[20:23], s87 offen offset:2048 sc1 lds\n"
      "v_mfma_f32_16x16x32_bf16 v[68:71], a[200:203], v[84:87], v[68:71]\n"
      "ds_read_b128 v[120:123], v5 offset:34816\n"
      "buffer_load_dwordx4 v5, s[20:23], s87 offen offset:3072 sc1 lds\n"
      "v_mfma_f32_16x16x32_bf16 v[72:75], a[200:203], v[92:95], v[72:75]\n"
      "ds_read_b128 v[128:131], v5 offset:35840\n"
      "s_waitcnt lgkmcnt(4)\n"
      "v_mfma_f32_16x16x32_bf16 v[60:63], a[204:207], v[96:99], v[60:63]\n"
      "ds_read_b128 v[76:79], v5 offset:36864\n"
      "s_add_u32 m0, m0, 16384\n"
      "v_mfma_f32_16x16x32_bf16 v[64:67], a[204:207], v[100:103], v[64:67]\n"
      "ds_read_b128 v[80:83], v5 offset:37888\n"
      "s_add_u32 s87, s87, 20480\n"
      "v_mfma_f32_16x16x32_bf16 v[68:71], a[204:207], v[104:107], v[68:71]\n"
      "ds_read_b128 v[84:87], v5 offset:38912\n"
      "buffer_load_dwordx4 v5, s[20:23], s87 offen sc1 lds\n"
      "v_mfma_f32_16x16x32_bf16 v[72:75], a[204:207], v[108:111], v[72:75]\n"
      "ds_read_b128 v[92:95], v5 offset:39936\n"
      "buffer_load_dwordx4 v5, s[20:23], s87 offen offset:1024 sc1 lds\n"
      "s_waitcnt lgkmcnt(4)\n"
      "v_mfma_f32_16x16x32_bf16 v[60:63], a[208:211], v[112:115], v[60:63]\n"
      "ds_read_b128 v[96:99], v5 offset:40960\n"
      "buffer_load_dwordx4 v5, s[20:23], s87 offen offset:2048 sc1 lds\n"
      "v_mfma_f32_16x16x32_bf16 v[64:67], a[208:211], v[116:119], v[64:67]\n"
      "ds_read_b128 v[100:103], v5 offset:41984\n"
      "buffer_load_dwordx4 v5, s[20:23], s87 offen offset:3072 sc1 lds\n"
      "v_mfma_f32_16x16x32_bf16 v[68:71], a[208:211], v[120:123], v[68:71]\n"
      "ds_read_b128 v[104:107], v5 offset:43008\n"
      "s_add_u32 m0, m0, 16384\n"
      "v_mfma_f32_16x16x32_bf16 v[72:75], a[208:211], v[128:131], v[72:75]\n"
      "ds_read_b128 v[108:111], v5 offset:44032\n"
      "s_waitcnt lgkmcnt(4)\n"
      "v_mfma_f32_16x16x32_bf16 v[60:63], a[212:215], v[76:79], v[60:63]\n"
      "ds_read_b128 v[112:115], v5 offset:45056\n"
      "s_add_u32 s87, s87, 20480\n"
      "v_mfma_f32_16x16x32_bf16 v[64:67], a[212:215], v[80:83], v[64:67]\n"
      "ds_read_b128 v[116:119], v5 offset:46080\n"
      "buffer_load_dwordx4 v5, s[20:23], s87 offen sc1 lds\n"
      "v_mfma_f32_16x16x32_bf16 v[68:71], a[212:215], v[84:87], v[68:71]\n"
      "ds_read_b128 v[120:123], v5 offset:47104\n"
      "buffer_load_dwordx4 v5, s[20:23], s87 offen offset:1024 sc1 lds\n"
      "v_mfma_f32_16x16x32_bf16 v[72:75], a[212:215], v[92:95], v[72:75]\n"
      "ds_read_b128 v[128:131], v5 offset:48128\n"
      "buffer_load_dwordx4 v5, s[20:23], s87 offen offset:2048 sc1 lds\n"
      "s_waitcnt vmcnt(15)\n"
      "s_barrier\n"
      "s_waitcnt lgkmcnt(4)\n"
      "v_mfma_f32_16x16x32_bf16 v[60:63], a[216:219], v[96:99], v[60:63]\n"
      "ds_read_b128 v[76:79], v5 offset:49152\n"
      "buffer_load_dwordx4 v5, s[20:23], s87 offen offset:3072 sc1 lds\n"
      "v_mfma_f32_16x16x32_bf16 v[64:67], a[216:219], v[100:103], v[64:67]\n"
      "ds_read_b128 v[80:83], v5 offset:50176\n"
      "v_mfma_f32_16x16x32_bf16 v[68:71], a[216:219], v[104:107], v[68:71]\n"
      "ds_read_b128 v[84:87], v5 offset:51200\n"
      "v_mfma_f32_16x16x32_bf16 v[72:75], a[216:219], v[108:111], v[72:75]\n"
      "ds_read_b128 v[92:95], v5 offset:52224\n"
      "s_waitcnt lgkmcnt(4)\n"
      "v_mfma_f32_16x16x32_bf16 v[60:63], a[220:223], v[112:115], v[60:63]\n"
      "ds_read_b128 v[96:99], v5 offset:53248\n"
      "s_add_u32 m0, m0, 16384\n"
      "v_mfma_f32_16x16x32_bf16 v[64:67], a[220:223], v[116:119], v[64:67]\n"
      "ds_read_b128 v[100:103], v5 offset:54272\n"
      "v_mfma_f32_16x16x32_bf16 v[68:71], a[220:223], v[120:123], v[68:71]\n"
      "s_add_u32 s87, s87, 20480\n"
      "ds_read_b128 v[104:107], v5 offset:55296\n"
      "v_mfma_f32_16x16x32_bf16 v[72:75], a[220:223], v[128:131], v[72:75]\n"
      "ds_read_b128 v[108:111], v5 offset:56320\n"
      "s_waitcnt lgkmcnt(4)\n"
      "v_mfma_f32_16x16x32_bf16 v[60:63], a[224:227], v[76:79], v[60:63]\n"
      "ds_read_b128 v[112:115], v5 offset:57344\n"
      "buffer_load_dwordx4 v5, s[20:23], s87 offen sc1 lds\n"
      "v_mfma_f32_16x16x32_bf16 v[64:67], a[224:227], v[80:83], v[64:67]\n"
      "ds_read_b128 v[116:119], v5 offset:58368\n"
      "v_mfma_f32_16x16x32_bf16 v[68:71], a[224:227], v[84:87], v[68:71]\n"
      "buffer_load_dwordx4 v5, s[20:23], s87 offen offset:1024 sc1 lds\n"
      "ds_read_b128 v[120:123], v5 offset:59392\n"
      "v_mfma_f32_16x16x32_bf16 v[72:75], a[224:227], v[92:95], v[72:75]\n"
      "ds_read_b128 v[128:131], v5 offset:60416\n"
      "s_waitcnt lgkmcnt(4)\n"
      "v_mfma_f32_16x16x32_bf16 v[60:63], a[228:231], v[96:99], v[60:63]\n"
      "ds_read_b128 v[76:79], v5 offset:61440\n"
      "buffer_load_dwordx4 v5, s[20:23], s87 offen offset:2048 sc1 lds\n"
      "v_mfma_f32_16x16x32_bf16 v[64:67], a[228:231], v[100:103], v[64:67]\n"
      "ds_read_b128 v[80:83], v5 offset:62464\n"
      "v_mfma_f32_16x16x32_bf16 v[68:71], a[228:231], v[104:107], v[68:71]\n"
      "buffer_load_dwordx4 v5, s[20:23], s87 offen offset:3072 sc1 lds\n"
      "ds_read_b128 v[84:87], v5 offset:63488\n"
      "v_mfma_f32_16x16x32_bf16 v[72:75], a[228:231], v[108:111], v[72:75]\n"
      "ds_read_b128 v[92:95], v5 offset:64512\n"
      "s_waitcnt vmcnt(16)\n"
      "s_barrier\n"
      "s_waitcnt lgkmcnt(4)\n"
      "v_mfma_f32_16x16x32_bf16 v[60:63], a[232:235], v[112:115], v[60:63]\n"
      "ds_read_b128 v[96:99], v56\n"
      "s_sub_u32 m0, m0, 131072\n"
      "v_mfma_f32_16x16x32_bf16 v[64:67], a[232:235], v[116:119], v[64:67]\n"
      "ds_read_b128 v[100:103], v56 offset:1024\n"
      "v_mfma_f32_16x16x32_bf16 v[68:71], a[232:235], v[120:123], v[68:71]\n"
      "ds_read_b128 v[104:107], v56 offset:2048\n"
      "v_mfma_f32_16x16x32_bf16 v[72:75], a[232:235], v[128:131], v[72:75]\n"
      "ds_read_b128 v[108:111], v56 offset:3072\n"
      "s_waitcnt lgkmcnt(4)\n"
      "v_mfma_f32_16x16x32_bf16 v[60:63], a[236:239], v[76:79], v[60:63]\n"
      "ds_read_b128 v[112:115], v56 offset:4096\n"
      "s_add_u32 s87, s87, 20480\n"
      "v_mfma_f32_16x16x32_bf16 v[64:67], a[236:239], v[80:83], v[64:67]\n"
      "ds_read_b128 v[116:119], v56 offset:5120\n"
      "v_mfma_f32_16x16x32_bf16 v[68:71], a[236:239], v[84:87], v[68:71]\n"
      "buffer_load_dwordx4 v5, s[20:23], s87 offen sc1 lds\n"
      "ds_read_b128 v[120:123], v56 offset:6144\n"
      "v_mfma_f32_16x16x32_bf16 v[72:75], a[236:239], v[92:95], v[72:75]\n"
      "ds_read_b128 v[128:131], v56 offset:7168\n"
      "s_waitcnt lgkmcnt(4)\n"
      "v_mfma_f32_16x16x32_bf16 v[60:63], a[240:243], v[96:99], v[60:63]\n"
      "ds_read_b128 v[76:79], v56 offset:8192\n"
      "buffer_load_dwordx4 v5, s[20:23], s87 offen offset:1024 sc1 lds\n"
      "v_mfma_f32_16x16x32_bf16 v[64:67], a[240:243], v[100:103], v[64:67]\n"
      "ds_read_b128 v[80:83], v56 offset:9216\n"
      "v_mfma_f32_16x16x32_bf16 v[68:71], a[240:243], v[104:107], v[68:71]\n"
      "buffer_load_dwordx4 v5, s[20:23], s87 offen offset:2048 sc1 lds\n"
      "ds_read_b128 v[84:87], v56 offset:10240\n"
      "v_mfma_f32_16x16x32_bf16 v[72:75], a[240:243], v[108:111], v[72:75]\n"
      "ds_read_b128 v[92:95], v56 offset:11264\n"
      "s_waitcnt lgkmcnt(4)\n"
      "v_mfma_f32_16x16x32_bf16 v[60:63], a[244:247], v[112:115], v[60:63]\n"
      "ds_read_b128 v[96:99], v56 offset:12288\n"
      "buffer_load_dwordx4 v5, s[20:23], s87 offen offset:3072 sc1 lds\n"
      "v_mfma_f32_16x16x32_bf16 v[64:67], a[244:247], v[116:119], v[64:67]\n"
      "ds_read_b128 v[100:103], v56 offset:13312\n"
      "v_mfma_f32_16x16x32_bf16 v[68:71], a[244:247], v[120:123], v[68:71]\n"
      "s_add_u32 m0, m0, 16384\n"
      "ds_read_b128 v[104:107], v56 offset:14336\n"
      "v_mfma_f32_16x16x32_bf16 v[72:75], a[244:247], v[128:131], v[72:75]\n"
      "ds_read_b128 v[108:111], v56 offset:15360\n"
      "s_waitcnt vmcnt(16)\n"
      "s_barrier\n"
      "s_waitcnt lgkmcnt(4)\n"
      "v_mfma_f32_16x16x32_bf16 v[60:63], a[248:251], v[76:79], v[60:63]\n"
      "ds_read_b128 v[112:115], v56 offset:16384\n"
      "s_add_u32 s82, s82, 4096\n"
      "v_mfma_f32_16x16x32_bf16 v[64:67], a[248:251], v[80:83], v[64:67]\n"
      "ds_read_b128 v[116:119], v56 offset:17408\n"
      "v_mfma_f32_16x16x32_bf16 v[68:71], a[248:251], v[84:87], v[68:71]\n"
      "ds_read_b128 v[120:123], v56 offset:18432\n"
      "v_mfma_f32_16x16x32_bf16 v[72:75], a[248:251], v[92:95], v[72:75]\n"
      "ds_read_b128 v[128:131], v56 offset:19456\n"
      "s_waitcnt lgkmcnt(4)\n"
      "v_mfma_f32_16x16x32_bf16 v[60:63], a[252:255], v[96:99], v[60:63]\n"
      "ds_read_b128 v[76:79], v56 offset:20480\n"
      "buffer_load_dwordx4 a[0:3], v5, s[20:23], s82 offen\n"
      "v_mfma_f32_16x16x32_bf16 v[64:67], a[252:255], v[100:103], v[64:67]\n"
      "ds_read_b128 v[80:83], v56 offset:21504\n"
      "v_mfma_f32_16x16x32_bf16 v[68:71], a[252:255], v[104:107], v[68:71]\n"
      "buffer_load_dwordx4 a[4:7], v5, s[20:23], s82 offen offset:1024\n"
      "ds_read_b128 v[84:87], v56 offset:22528\n"
      "v_mfma_f32_16x16x32_bf16 v[72:75], a[252:255], v[108:111], v[72:75]\n"
      "ds_read_b128 v[92:95], v56 offset:23552\n"
      "s_waitcnt lgkmcnt(4)\n"
      "v_mfma_f32_16x16x32_bf16 v[60:63], v[8:11], v[112:115], v[60:63]\n"
      "ds_read_b128 v[96:99], v56 offset:24576\n"
      "buffer_load_dwordx4 a[8:11], v5, s[20:23], s82 offen offset:2048\n"
      "v_mfma_f32_16x16x32_bf16 v[64:67], v[8:11], v[116:119], v[64:67]\n"
      "ds_read_b128 v[100:103], v56 offset:25600\n"
      "v_mfma_f32_16x16x32_bf16 v[68:71], v[8:11], v[120:123], v[68:71]\n"
      "s_add_u32 s87, s87, 20480\n"
      "ds_read_b128 v[104:107], v56 offset:26624\n"
      "v_mfma_f32_16x16x32_bf16 v[72:75], v[8:11], v[128:131], v[72:75]\n"
      "ds_read_b128 v[108:111], v56 offset:27648\n"
      "s_waitcnt lgkmcnt(4)\n"
      "v_mfma_f32_16x16x32_bf16 v[60:63], v[12:15], v[76:79], v[60:63]\n"
      "ds_read_b128 v[112:115], v56 offset:28672\n"
      "buffer_load_dwordx4 a[12:15], v5, s[20:23], s82 offen offset:3072\n"
      "v_mfma_f32_16x16x32_bf16 v[64:67], v[12:15], v[80:83], v[64:67]\n"
      "ds_read_b128 v[116:119], v56 offset:29696\n"
      "v_mfma_f32_16x16x32_bf16 v[68:71], v[12:15], v[84:87], v[68:71]\n"
      "buffer_load_dwordx4 v5, s[20:23], s87 offen sc1 lds\n"
      "ds_read_b128 v[120:123], v56 offset:30720\n"
      "v_mfma_f32_16x16x32_bf16 v[72:75], v[12:15], v[92:95], v[72:75]\n"
      "ds_read_b128 v[128:131], v56 offset:31744\n"
      "s_waitcnt vmcnt(17)\n"
      "s_barrier\n"
      "s_waitcnt lgkmcnt(4)\n"
      "v_mfma_f32_16x16x32_bf16 v[60:63], v[16:19], v[96:99], v[60:63]\n"
      "ds_read_b128 v[76:79], v56 offset:32768\n"
      "s_add_u32 s82, s82, 4096\n"
      "v_mfma_f32_16x16x32_bf16 v[64:67], v[16:19], v[100:103], v[64:67]\n"
      "ds_read_b128 v[80:83], v56 offset:33792\n"
      "v_mfma_f32_16x16x32_bf16 v[68:71], v[16:19], v[104:107], v[68:71]\n"
      "ds_read_b128 v[84:87], v56 offset:34816\n"
      "v_mfma_f32_16x16x32_bf16 v[72:75], v[16:19], v[108:111], v[72:75]\n"
      "ds_read_b128 v[92:95], v56 offset:35840\n"
      "s_waitcnt lgkmcnt(4)\n"
      "v_mfma_f32_16x16x32_bf16 v[60:63], v[20:23], v[112:115], v[60:63]\n"
      "ds_read_b128 v[96:99], v56 offset:36864\n"
      "buffer_load_dwordx4 a[16:19], v5, s[20:23], s82 offen\n"
      "v_mfma_f32_16x16x32_bf16 v[64:67], v[20:23], v[116:119], v[64:67]\n"
      "ds_read_b128 v[100:103], v56 offset:37888\n"
      "v_mfma_f32_16x16x32_bf16 v[68:71], v[20:23], v[120:123], v[68:71]\n"
      "buffer_load_dwordx4 a[20:23], v5, s[20:23], s82 offen offset:1024\n"
      "ds_read_b128 v[104:107], v56 offset:38912\n"
      "v_mfma_f32_16x16x32_bf16 v[72:75], v[20:23], v[128:131], v[72:75]\n"
      "ds_read_b128 v[108:111], v56 offset:39936\n"
      "s_waitcnt lgkmcnt(4)\n"
      "v_mfma_f32_16x16x32_bf16 v[60:63], v[24:27], v[76:79], v[60:63]\n"
      "ds_read_b128 v[112:115], v56 offset:40960\n"
      "buffer_load_dwordx4 a[24:27], v5, s[20:23], s82 offen offset:2048\n"
      "v_mfma_f32_16x16x32_bf16 v[64:67], v[24:27], v[80:83], v[64:67]\n"
      "ds_read_b128 v[116:119], v56 offset:41984\n"
      "v_mfma_f32_16x16x32_bf16 v[68:71], v[24:27], v[84:87], v[68:71]\n"
      "buffer_load_dwordx4 v5, s[20:23], s87 offen offset:1024 sc1 lds\n"
      "ds_read_b128 v[120:123], v56 offset:43008\n"
      "v_mfma_f32_16x16x32_bf16 v[72:75], v[24:27], v[92:95], v[72:75]\n"
      "ds_read_b128 v[128:131], v56 offset:44032\n"
      "s_waitcnt lgkmcnt(4)\n"
      "v_mfma_f32_16x16x32_bf16 v[60:63], v[28:31], v[96:99], v[60:63]\n"
      "ds_read_b128 v[76:79], v56 offset:45056\n"
      "buffer_load_dwordx4 a[28:31], v5, s[20:23], s82 offen offset:3072\n"
      "v_mfma_f32_16x16x32_bf16 v[64:67], v[28:31], v[100:103], v[64:67]\n"
      "ds_read_b128 v[80:83], v56 offset:46080\n"
      "v_mfma_f32_16x16x32_bf16 v[68:71], v[28:31], v[104:107], v[68:71]\n"
      "buffer_load_dwordx4 v5, s[20:23], s87 offen offset:2048 sc1 lds\n"
      "ds_read_b128 v[84:87], v56 offset:47104\n"
      "v_mfma_f32_16x16x32_bf16 v[72:75], v[28:31], v[108:111], v[72:75]\n"
      "ds_read_b128 v[92:95], v56 offset:48128\n"
      "s_waitcnt vmcnt(19)\n"
      "s_barrier\n"
      "s_waitcnt lgkmcnt(4)\n"
      "v_mfma_f32_16x16x32_bf16 v[60:63], v[32:35], v[112:115], v[60:63]\n"
      "ds_read_b128 v[96:99], v56 offset:49152\n"
      "s_add_u32 s82, s82, 4096\n"
      "v_mfma_f32_16x16x32_bf16 v[64:67], v[32:35], v[116:119], v[64:67]\n"
      "ds_read_b128 v[100:103], v56 offset:50176\n"
      "v_mfma_f32_16x16x32_bf16 v[68:71], v[32:35], v[120:123], v[68:71]\n"
      "ds_read_b128 v[104:107], v56 offset:51200\n"
      "v_mfma_f32_16x16x32_bf16 v[72:75], v[32:35], v[128:131], v[72:75]\n"
      "ds_read_b128 v[108:111], v56 offset:52224\n"
      "s_waitcnt lgkmcnt(4)\n"
      "v_mfma_f32_16x16x32_bf16 v[60:63], v[36:39], v[76:79], v[60:63]\n"
      "ds_read_b128 v[112:115], v56 offset:53248\n"
      "buffer_load_dwordx4 a[32:35], v5, s[20:23], s82 offen\n"
      "v_mfma_f32_16x16x32_bf16 v[64:67], v[36:39], v[80:83], v[64:67]\n"
      "ds_read_b128 v[116:119], v56 offset:54272\n"
      "v_mfma_f32_16x16x32_bf16 v[68:71], v[36:39], v[84:87], v[68:71]\n"
      "buffer_load_dwordx4 a[36:39], v5, s[20:23], s82 offen offset:1024\n"
      "ds_read_b128 v[120:123], v56 offset:55296\n"
      "v_mfma_f32_16x16x32_bf16 v[72:75], v[36:39], v[92:95], v[72:75]\n"
      "ds_read_b128 v[128:131], v56 offset:56320\n"
      "s_waitcnt lgkmcnt(4)\n"
      "v_mfma_f32_16x16x32_bf16 v[60:63], v[40:43], v[96:99], v[60:63]\n"
      "ds_read_b128 v[76:79], v56 offset:57344\n"
      "buffer_load_dwordx4 a[40:43], v5, s[20:23], s82 offen offset:2048\n"
      "v_mfma_f32_16x16x32_bf16 v[64:67], v[40:43], v[100:103], v[64:67]\n"
      "ds_read_b128 v[80:83], v56 offset:58368\n"
      "v_mfma_f32_16x16x32_bf16 v[68:71], v[40:43], v[104:107], v[68:71]\n"
      "buffer_load_dwordx4 v5, s[20:23], s87 offen offset:3072 sc1 lds\n"
      "ds_read_b128 v[84:87], v56 offset:59392\n"
      "v_mfma_f32_16x16x32_bf16 v[72:75], v[40:43], v[108:111], v[72:75]\n"
      "ds_read_b128 v[92:95], v56 offset:60416\n"
      "s_waitcnt lgkmcnt(4)\n"
      "v_mfma_f32_16x16x32_bf16 v[60:63], v[44:47], v[112:115], v[60:63]\n"
      "ds_read_b128 v[96:99], v56 offset:61440\n"
      "buffer_load_dwordx4 a[44:47], v5, s[20:23], s82 offen offset:3072\n"
      "v_mfma_f32_16x16x32_bf16 v[64:67], v[44:47], v[116:119], v[64:67]\n"
      "ds_read_b128 v[100:103], v56 offset:62464\n"
      "v_mfma_f32_16x16x32_bf16 v[68:71], v[44:47], v[120:123], v[68:71]\n"
      "s_add_u32 s82, s82, 4096\n"
      "ds_read_b128 v[104:107], v56 offset:63488\n"
      "v_mfma_f32_16x16x32_bf16 v[72:75], v[44:47], v[128:131], v[72:75]\n"
      "ds_read_b128 v[108:111], v56 offset:64512\n"
      "s_waitcnt vmcnt(15)\n"
      "s_barrier\n"
      "s_waitcnt lgkmcnt(4)\n"
      "v_mfma_f32_16x16x32_bf16 v[60:63], a[0:3], v[76:79], v[60:63]\n"
      "ds_read_b128 v[112:115], v57\n"
      "buffer_load_dwordx4 a[48:51], v5, s[20:23], s82 offen\n"
      "v_mfma_f32_16x16x32_bf16 v[64:67], a[0:3], v[80:83], v[64:67]\n"
      "ds_read_b128 v[116:119], v57 offset:1024\n"
      "v_mfma_f32_16x16x32_bf16 v[68:71], a[0:3], v[84:87], v[68:71]\n"
      "ds_read_b128 v[120:123], v57 offset:2048\n"
      "v_mfma_f32_16x16x32_bf16 v[72:75], a[0:3], v[92:95], v[72:75]\n"
      "ds_read_b128 v[128:131], v57 offset:3072\n"
      "s_waitcnt vmcnt(15)\n"
      "s_waitcnt lgkmcnt(4)\n"
      "v_mfma_f32_16x16x32_bf16 v[60:63], a[4:7], v[96:99], v[60:63]\n"
      "ds_read_b128 v[76:79], v57 offset:4096\n"
      "v_mfma_f32_16x16x32_bf16 v[64:67], a[4:7], v[100:103], v[64:67]\n"
      "ds_read_b128 v[80:83], v57 offset:5120\n"
      "v_mfma_f32_16x16x32_bf16 v[68:71], a[4:7], v[104:107], v[68:71]\n"
      "ds_read_b128 v[84:87], v57 offset:6144\n"
      "v_mfma_f32_16x16x32_bf16 v[72:75], a[4:7], v[108:111], v[72:75]\n"
      "ds_read_b128 v[92:95], v57 offset:7168\n"
      "s_waitcnt vmcnt(14)\n"
      "s_waitcnt lgkmcnt(4)\n"
      "v_mfma_f32_16x16x32_bf16 v[60:63], a[8:11], v[112:115], v[60:63]\n"
      "ds_read_b128 v[96:99], v57 offset:8192\n"
      "v_mfma_f32_16x16x32_bf16 v[64:67], a[8:11], v[116:119], v[64:67]\n"
      "ds_read_b128 v[100:103], v57 offset:9216\n"
      "v_mfma_f32_16x16x32_bf16 v[68:71], a[8:11], v[120:123], v[68:71]\n"
      "ds_read_b128 v[104:107], v57 offset:10240\n"
      "v_mfma_f32_16x16x32_bf16 v[72:75], a[8:11], v[128:131], v[72:75]\n"
      "ds_read_b128 v[108:111], v57 offset:11264\n"
      "s_waitcnt vmcnt(13)\n"
      "s_waitcnt lgkmcnt(4)\n"
      "v_mfma_f32_16x16x32_bf16 v[60:63], a[12:15], v[76:79], v[60:63]\n"
      "ds_read_b128 v[112:115], v57 offset:12288\n"
      "v_mfma_f32_16x16x32_bf16 v[64:67], a[12:15], v[80:83], v[64:67]\n"
      "ds_read_b128 v[116:119], v57 offset:13312\n"
      "v_mfma_f32_16x16x32_bf16 v[68:71], a[12:15], v[84:87], v[68:71]\n"
      "ds_read_b128 v[120:123], v57 offset:14336\n"
      "v_mfma_f32_16x16x32_bf16 v[72:75], a[12:15], v[92:95], v[72:75]\n"
      "ds_read_b128 v[128:131], v57 offset:15360\n"
      "s_waitcnt vmcnt(11)\n"
      "s_barrier\n"
      "s_waitcnt lgkmcnt(4)\n"
      "v_mfma_f32_16x16x32_bf16 v[60:63], a[16:19], v[96:99], v[60:63]\n"
      "ds_read_b128 v[76:79], v5\n"
      "v_mfma_f32_16x16x32_bf16 v[64:67], a[16:19], v[100:103], v[64:67]\n"
      "ds_read_b128 v[80:83], v5 offset:1024\n"
      "v_mfma_f32_16x16x32_bf16 v[68:71], a[16:19], v[104:107], v[68:71]\n"
      "ds_read_b128 v[84:87], v5 offset:2048\n"
      "v_mfma_f32_16x16x32_bf16 v[72:75], a[16:19], v[108:111], v[72:75]\n"
      "ds_read_b128 v[92:95], v5 offset:3072\n"
      "s_waitcnt vmcnt(10)\n"
      "s_waitcnt lgkmcnt(4)\n"
      "v_mfma_f32_16x16x32_bf16 v[60:63], a[20:23], v[112:115], v[60:63]\n"
      "ds_read_b128 v[96:99], v5 offset:4096\n"
      "v_mfma_f32_16x16x32_bf16 v[64:67], a[20:23], v[116:119], v[64:67]\n"
      "ds_read_b128 v[100:103], v5 offset:5120\n"
      "v_mfma_f32_16x16x32_bf16 v[68:71], a[20:23], v[120:123], v[68:71]\n"
      "ds_read_b128 v[104:107], v5 offset:6144\n"
      "v_mfma_f32_16x16x32_bf16 v[72:75], a[20:23], v[128:131], v[72:75]\n"
      "ds_read_b128 v[108:111], v5 offset:7168\n"
      "s_waitcnt vmcnt(9)\n"
      "s_waitcnt lgkmcnt(4)\n"
      "v_mfma_f32_16x16x32_bf16 v[60:63], a[24:27], v[76:79], v[60:63]\n"
      "ds_read_b128 v[112:115], v5 offset:8192\n"
      "v_mfma_f32_16x16x32_bf16 v[64:67], a[24:27], v[80:83], v[64:67]\n"
      "ds_read_b128 v[116:119], v5 offset:9216\n"
      "v_mfma_f32_16x16x32_bf16 v[68:71], a[24:27], v[84:87], v[68:71]\n"
      "ds_read_b128 v[120:123], v5 offset:10240\n"
      "v_mfma_f32_16x16x32_bf16 v[72:75], a[24:27], v[92:95], v[72:75]\n"
      "ds_read_b128 v[128:131], v5 offset:11264\n"
      "s_waitcnt vmcnt(7)\n"
      "s_waitcnt lgkmcnt(4)\n"
      "v_mfma_f32_16x16x32_bf16 v[60:63], a[28:31], v[96:99], v[60:63]\n"
      "ds_read_b128 v[76:79], v5 offset:12288\n"
      "v_mfma_f32_16x16x32_bf16 v[64:67], a[28:31], v[100:103], v[64:67]\n"
      "ds_read_b128 v[80:83], v5 offset:13312\n"
      "v_mfma_f32_16x16x32_bf16 v[68:71], a[28:31], v[104:107], v[68:71]\n"
      "ds_read_b128 v[84:87], v5 offset:14336\n"
      "v_mfma_f32_16x16x32_bf16 v[72:75], a[28:31], v[108:111], v[72:75]\n"
      "ds_read_b128 v[92:95], v5 offset:15360\n"
      "s_waitcnt vmcnt(5)\n"
      "s_waitcnt vmcnt(2)\n"
      "s_barrier\n"
      "s_waitcnt lgkmcnt(4)\n"
      "v_mfma_f32_16x16x32_bf16 v[60:63], a[32:35], v[112:115], v[60:63]\n"
      "ds_read_b128 v[96:99], v5 offset:16384\n"
      "v_mfma_f32_16x16x32_bf16 v[64:67], a[32:35], v[116:119], v[64:67]\n"
      "ds_read_b128 v[100:103], v5 offset:17408\n"
      "v_mfma_f32_16x16x32_bf16 v[68:71], a[32:35], v[120:123], v[68:71]\n"
      "ds_read_b128 v[104:107], v5 offset:18432\n"
      "v_mfma_f32_16x16x32_bf16 v[72:75], a[32:35], v[128:131], v[72:75]\n"
      "ds_read_b128 v[108:111], v5 offset:19456\n"
      "s_waitcnt lgkmcnt(4)\n"
      "v_mfma_f32_16x16x32_bf16 v[60:63], a[36:39], v[76:79], v[60:63]\n"
      "ds_read_b128 v[112:115], v5 offset:20480\n"
      "v_mfma_f32_16x16x32_bf16 v[64:67], a[36:39], v[80:83], v[64:67]\n"
      "ds_read_b128 v[116:119], v5 offset:21504\n"
      "v_mfma_f32_16x16x32_bf16 v[68:71], a[36:39], v[84:87], v[68:71]\n"
      "ds_read_b128 v[120:123], v5 offset:22528\n"
      "v_mfma_f32_16x16x32_bf16 v[72:75], a[36:39], v[92:95], v[72:75]\n"
      "ds_read_b128 v[128:131], v5 offset:23552\n"
      "s_waitcnt lgkmcnt(4)\n"
      "v_mfma_f32_16x16x32_bf16 v[60:63], a[40:43], v[96:99], v[60:63]\n"
      "ds_read_b128 v[76:79], v5 offset:24576\n"
      "v_mfma_f32_16x16x32_bf16 v[64:67], a[40:43], v[100:103], v[64:67]\n"
      "ds_read_b128 v[80:83], v5 offset:25600\n"
      "v_mfma_f32_16x16x32_bf16 v[68:71], a[40:43], v[104:107], v[68:71]\n"
      "ds_read_b128 v[84:87], v5 offset:26624\n"
      "v_mfma_f32_16x16x32_bf16 v[72:75], a[40:43], v[108:111], v[72:75]\n"
      "ds_read_b128 v[92:95], v5 offset:27648\n"
      "s_waitcnt vmcnt(1)\n"
      "s_waitcnt lgkmcnt(4)\n"
      "v_mfma_f32_16x16x32_bf16 v[60:63], a[44:47], v[112:115], v[60:63]\n"
      "v_mfma_f32_16x16x32_bf16 v[64:67], a[44:47], v[116:119], v[64:67]\n"
      "v_mfma_f32_16x16x32_bf16 v[68:71], a[44:47], v[120:123], v[68:71]\n"
      "v_mfma_f32_16x16x32_bf16 v[72:75], a[44:47], v[128:131], v[72:75]\n"
      "s_waitcnt vmcnt(0)\n"
      "s_waitcnt lgkmcnt(0)\n"
      "v_mfma_f32_16x16x32_bf16 v[60:63], a[48:51], v[76:79], v[60:63]\n"
      "v_mfma_f32_16x16x32_bf16 v[64:67], a[48:51], v[80:83], v[64:67]\n"
      "v_mfma_f32_16x16x32_bf16 v[68:71], a[48:51], v[84:87], v[68:71]\n"
      "v_mfma_f32_16x16x32_bf16 v[72:75], a[48:51], v[92:95], v[72:75]\n"
      "; P4 epilogue\n"
      "v_lshlrev_b32 v76, 16, v52\n"
      "v_and_b32 v77, 0xffff0000, v52\n"
      "v_lshlrev_b32 v78, 16, v53\n"
      "v_and_b32 v79, 0xffff0000, v53\n"
      "v_lshlrev_b32 v80, 16, v54\n"
      "v_and_b32 v81, 0xffff0000, v54\n"
      "v_lshlrev_b32 v82, 16, v55\n"
      "v_and_b32 v83, 0xffff0000, v55\n"
      "v_lshlrev_b32 v84, 16, v88\n"
      "v_and_b32 v85, 0xffff0000, v88\n"
      "v_lshlrev_b32 v86, 16, v89\n"
      "v_and_b32 v87, 0xffff0000, v89\n"
      "v_lshlrev_b32 v92, 16, v90\n"
      "v_and_b32 v93, 0xffff0000, v90\n"
      "v_lshlrev_b32 v94, 16, v91\n"
      "v_and_b32 v95, 0xffff0000, v91\n"
      "v_pk_fma_f32 v[60:61], v[124:125], v[60:61], v[76:77] op_sel:[0,0,0] op_sel_hi:[1,1,1]\n"
      "v_pk_fma_f32 v[62:63], v[126:127], v[62:63], v[78:79] op_sel:[0,0,0] op_sel_hi:[1,1,1]\n"
      "v_pk_fma_f32 v[64:65], v[124:125], v[64:65], v[80:81] op_sel:[0,0,0] op_sel_hi:[1,1,1]\n"
      "v_pk_fma_f32 v[66:67], v[126:127], v[66:67], v[82:83] op_sel:[0,0,0] op_sel_hi:[1,1,1]\n"
      "v_pk_fma_f32 v[68:69], v[124:125], v[68:69], v[84:85] op_sel:[0,0,0] op_sel_hi:[1,1,1]\n"
      "v_pk_fma_f32 v[70:71], v[126:127], v[70:71], v[86:87] op_sel:[0,0,0] op_sel_hi:[1,1,1]\n"
      "v_pk_fma_f32 v[72:73], v[124:125], v[72:73], v[92:93] op_sel:[0,0,0] op_sel_hi:[1,1,1]\n"
      "v_pk_fma_f32 v[74:75], v[126:127], v[74:75], v[94:95] op_sel:[0,0,0] op_sel_hi:[1,1,1]\n"
      "v_and_b32 v50, 1, v49\n"
      "v_lshlrev_b32 v50, 15, v50\n"
      "v_lshl_add_u32 v50, v48, 11, v50\n"
      "v_lshrrev_b32 v51, 1, v49\n"
      "v_lshl_add_u32 v50, v51, 4, v50\n"
      "s_lshl_b32 s83, s37, 7\n"
      "s_add_u32 s83, s83, s38\n"
      "s_lshl_b32 s83, s83, 10\n"
      "s_lshl_b32 s84, s29, 7\n"
      "s_add_u32 s83, s83, s84\n"
      "s_lshl_b32 s84, s30, 5\n"
      "s_add_u32 s76, s83, s84\n"
      "s_add_u32 s77, s76, 65536\n"
      "v_cvt_pk_bf16_f32 v96, v60, v61\n"
      "v_cvt_pk_bf16_f32 v97, v62, v63\n"
      "v_cvt_pk_bf16_f32 v98, v64, v65\n"
      "v_cvt_pk_bf16_f32 v99, v66, v67\n"
      "v_cvt_pk_bf16_f32 v100, v68, v69\n"
      "v_cvt_pk_bf16_f32 v101, v70, v71\n"
      "v_cvt_pk_bf16_f32 v102, v72, v73\n"
      "v_cvt_pk_bf16_f32 v103, v74, v75\n"
      "v_permlane16_swap_b32 v96, v98\n"
      "v_permlane16_swap_b32 v97, v99\n"
      "v_permlane16_swap_b32 v100, v102\n"
      "v_permlane16_swap_b32 v101, v103\n"
      "buffer_store_dwordx4 v[96:99], v50, s[24:27], s76 offen\n"
      "buffer_store_dwordx4 v[100:103], v50, s[24:27], s77 offen\n"
      "s_cmp_lg_u32 s34, 0\n"
      "s_cbranch_scc1 Lep_%=\n"
      "s_cmp_lg_u32 s30, 0\n"
      "s_cbranch_scc1 Lep_%=\n"
      "s_mov_b32 s83, 5312512\n"
      "s_mov_b32 s86, 0\n"
      "Lepq_%=:\n"
      "buffer_load_dwordx4 v[104:107], v5, s[20:23], s83 offen sc1\n"
      "s_add_u32 s86, s86, 1\n"
      "s_waitcnt vmcnt(0)\n"
      "v_xor_b32 v104, s31, v104\n"
      "v_xor_b32 v105, s31, v105\n"
      "v_xor_b32 v106, s31, v106\n"
      "v_xor_b32 v107, s31, v107\n"
      "v_or3_b32 v104, v104, v105, v106\n"
      "v_or_b32 v104, v104, v107\n"
      "v_cmp_ne_u32 vcc, 0, v104\n"
      "s_cmp_gt_u32 s86, 0x10000\n"
      "s_cbranch_scc1 Lepr_%=\n"
      "s_nop 3\n"
      "s_cbranch_vccnz Lepq_%=\n"
      "Lepr_%=:\n"
      "v_mov_b32 v104, 0\n"
      "v_mov_b32 v105, s31\n"
      "s_mov_b32 s83, 5314560\n"
      "s_mov_b64 exec, 1\n"
      "buffer_store_dword v105, v104, s[20:23], s83 offen sc1\n"
      "s_mov_b64 exec, -1\n"
      "s_nop 0\n"
      "Lep_%=:\n"
      "s_waitcnt vmcnt(0) lgkmcnt(0)\n"
      :
      : "s"(__builtin_amdgcn_kernarg_segment_ptr()), "s"((int)blockIdx.x), "v"((int)threadIdx.x)
      : "v4", "v5", "v6", "v7", "v8", "v9", "v10", "v11", "v12", "v13", "v14", "v15", "v16", "v17", "v18", "v19", "v20", "v21", "v22", "v23", "v24", "v25", "v26", "v27", "v28", "v29", "v30", "v31", "v32", "v33", "v34", "v35", "v36", "v37", "v38", "v39", "v40", "v41", "v42", "v43", "v44", "v45", "v46", "v47", "v48", "v49", "v50", "v51", "v52", "v53", "v54", "v55", "v56", "v57", "v58", "v59", "v60", "v61", "v62", "v63", "v64", "v65", "v66", "v67", "v68", "v69", "v70", "v71", "v72", "v73", "v74", "v75", "v76", "v77", "v78", "v79", "v80", "v81", "v82", "v83", "v84", "v85", "v86", "v87", "v88", "v89", "v90", "v91", "v92", "v93", "v94", "v95", "v96", "v97", "v98", "v99", "v100", "v101", "v102", "v103", "v104", "v105", "v106", "v107", "v108", "v109", "v110", "v111", "v112", "v113", "v114", "v115", "v116", "v117", "v118", "v119", "v120", "v121", "v122", "v123", "v124", "v125", "v126", "v127", "v128", "v129", "v130", "v131", "v132", "v133", "v134", "v135", "v136", "v137", "v138", "v139", "v140", "v141", "v142", "v143", "v144", "v145", "v146", "v147", "v148", "v149", "v150", "v151", "v152", "v153", "v154", "v155", "v156", "v157", "v158", "v159", "v160", "v161", "v162", "v163", "v164", "v165", "v166", "v167", "v168", "v169", "v170", "v171", "v172", "v173", "v174", "v175", "v176", "v177", "v178", "v179", "v180", "v181", "v182", "v183", "v184", "v185", "v186", "v187", "v188", "v189", "v190", "v191", "v192", "v193", "v194", "v195", "v196", "v197", "v198", "v199", "v200", "v201", "v202", "v203", "v204", "v205", "v206", "v207", "v208", "v209", "v210", "v211", "v212", "v213", "v214", "v215", "v216", "v217", "v218", "v219", "v220", "v221", "v222", "v223", "v224", "v225", "v226", "v227", "v228", "v229", "v230", "v231", "v232", "v233", "v234", "v235", "v236", "v237", "v238", "v239", "v240", "v241", "v242", "v243", "v244", "v245", "v246", "v247", "v248", "v249", "v250", "v251", "v252", "v253", "v254", "v255", "a0", "a1", "a2", "a3", "a4", "a5", "a6", "a7", "a8", "a9", "a10", "a11", "a12", "a13", "a14", "a15", "a16", "a17", "a18", "a19", "a20", "a21", "a22", "a23", "a24", "a25", "a26", "a27", "a28", "a29", "a30", "a31", "a32", "a33", "a34", "a35", "a36", "a37", "a38", "a39", "a40", "a41", "a42", "a43", "a44", "a45", "a46", "a47", "a48", "a49", "a50", "a51", "a52", "a53", "a54", "a55", "a56", "a57", "a58", "a59", "a60", "a61", "a62", "a63", "a64", "a65", "a66", "a67", "a68", "a69", "a70", "a71", "a72", "a73", "a74", "a75", "a76", "a77", "a78", "a79", "a80", "a81", "a82", "a83", "a84", "a85", "a86", "a87", "a88", "a89", "a90", "a91", "a92", "a93", "a94", "a95", "a96", "a97", "a98", "a99", "a100", "a101", "a102", "a103", "a104", "a105", "a106", "a107", "a108", "a109", "a110", "a111", "a112", "a113", "a114", "a115", "a116", "a117", "a118", "a119", "a120", "a121", "a122", "a123", "a124", "a125", "a126", "a127", "a128", "a129", "a130", "a131", "a132", "a133", "a134", "a135", "a136", "a137", "a138", "a139", "a140", "a141", "a142", "a143", "a144", "a145", "a146", "a147", "a148", "a149", "a150", "a151", "a152", "a153", "a154", "a155", "a156", "a157", "a158", "a159", "a160", "a161", "a162", "a163", "a164", "a165", "a166", "a167", "a168", "a169", "a170", "a171", "a172", "a173", "a174", "a175", "a176", "a177", "a178", "a179", "a180", "a181", "a182", "a183", "a184", "a185", "a186", "a187", "a188", "a189", "a190", "a191", "a192", "a193", "a194", "a195", "a196", "a197", "a198", "a199", "a200", "a201", "a202", "a203", "a204", "a205", "a206", "a207", "a208", "a209", "a210", "a211", "a212", "a213", "a214", "a215", "a216", "a217", "a218", "a219", "a220", "a221", "a222", "a223", "a224", "a225", "a226", "a227", "a228", "a229", "a230", "a231", "a232", "a233", "a234", "a235", "a236", "a237", "a238", "a239", "a240", "a241", "a242", "a243", "a244", "a245", "a246", "a247", "a248", "a249", "a250", "a251", "a252", "a253", "a254", "a255", "s4", "s5", "s6", "s7", "s8", "s9", "s10", "s11", "s12", "s13", "s14", "s15", "s16", "s17", "s18", "s19", "s20", "s21", "s22", "s23", "s24", "s25", "s26", "s27", "s28", "s29", "s30", "s31", "s34", "s35", "s36", "s37", "s38", "s39", "s40", "s41", "s42", "s43", "s44", "s45", "s46", "s47", "s48", "s49", "s50", "s51", "s52", "s53", "s54", "s55", "s56", "s57", "s58", "s59", "s60", "s61", "s62", "s63", "s64", "s65", "s66", "s67", "s68", "s69", "s70", "s71", "s72", "s73", "s74", "s75", "s76", "s77", "s78", "s79", "s80", "s81", "s82", "s83", "s84", "s85", "s86", "s87", "s88", "s89", "s90", "s91", "s92", "s93", "s94", "s95", "s96", "s97", "s98", "s99", "vcc", "scc", "memory");
}

extern "C" void kernel_launch(void* const* d_in, const int* in_sizes, int n_in, void* d_out, int out_size, void* d_ws,
                              size_t ws_size, hipStream_t stream) {
  Ptrs p;
  p.x = d_in[0]; p.wqkv = d_in[1]; p.wo = d_in[2]; p.w1 = d_in[4]; p.w2 = d_in[5]; p.c = d_in[6]; p.wmod = d_in[7];
  p.out = d_out; p.ws = d_ws;
  (void)hipFuncSetAttribute((const void*)k_mega, hipFuncAttributeMaxDynamicSharedMemorySize, LDS_BYTES);
  k_mega<<<256, 256, LDS_BYTES, stream>>>(p);
}
```
